```python
import torch

def _softmax(x, axis=-1):
    return torch.softmax(x, dim=axis)
def _gelu(x):
    return torch.nn.functional.gelu(x, approximate="tanh")
def _silu(x):
    return torch.nn.functional.silu(x)
def _sigmoid(x):
    return torch.sigmoid(x)
def _layernorm(x, axis=-1, eps=1e-5):
    m = x.mean(dim=axis, keepdim=True)
    v = x.var(dim=axis, keepdim=True, unbiased=False)
    return (x - m) / torch.sqrt(v + eps)
def _scatter_add(tgt, idx, src, axis=0):
    return tgt.clone().index_add_(axis, idx, src)
def _segment_sum(src, seg, n):
    out = torch.zeros((n,) + src.shape[1:], dtype=src.dtype)
    return out.index_add_(0, seg, src)
def _pad(x, pw):
    flat = tuple(v for pair in reversed(pw) for v in pair)
    return torch.nn.functional.pad(x, flat)

kernel_name = 'sequential_preLN__sliding_block_cached__glu_gated_swiglu__decode'

def setup_inputs():

    _w = (lambda a: (lambda t: t.to(torch.bfloat16) if t.is_floating_point() else t)(torch.as_tensor(a)))
    def _A_setup():
        B, Sq, D = 2, 1, 576
        Hq, Hkv, Sk, Dh = 32, 16, 32768, 128
        assert Hq % Hkv == 0, (Hq, Hkv)
        return {
            "x": _w((lambda *s: torch.randn(*s, dtype=torch.bfloat16) * 0.05)(B, Sq, D)),
            "k_cache": _w((lambda *s: torch.randn(*s, dtype=torch.bfloat16) * 1.6)(B, Hkv, Sk, Dh)),
            "v_cache": _w((lambda *s: torch.randn(*s, dtype=torch.bfloat16) * 0.8)(B, Hkv, Sk, Dh)),
            "wq": _w((lambda *s: torch.randn(*s, dtype=torch.bfloat16) * 1.6)(D, Hq * Dh) * (D ** -0.5)),
            "wo": _w((lambda *s: torch.randn(*s, dtype=torch.bfloat16) * 0.8)(Hq * Dh, D) * ((Hq * Dh) ** -0.5)),
        }
    _A_in = _A_setup()

    def _B_setup():
        B, S, D, F = 2, 1, 576, 4608
        return {
            "x": _w((lambda *s: torch.randn(*s, dtype=torch.bfloat16) * 0.05)(B, S, D)),
            "w1": _w((lambda *s: torch.randn(*s, dtype=torch.bfloat16) * 0.4)(D, F) * (2.0 / D) ** 0.5),
            "w3": _w((lambda *s: torch.randn(*s, dtype=torch.bfloat16) * 0.4)(D, F) * (2.0 / D) ** 0.5),
            "w2": _w((lambda *s: torch.randn(*s, dtype=torch.bfloat16) * 0.4)(F, D) * (2.0 / F) ** 0.5),
        }
    _B_in = _B_setup()

    return {
        "A_x": _A_in["x"],
        "A_k_cache": _A_in["k_cache"],
        "A_v_cache": _A_in["v_cache"],
        "A_wq": _A_in["wq"],
        "A_wo": _A_in["wo"],
        "B_x": _B_in["x"],
        "B_w1": _B_in["w1"],
        "B_w3": _B_in["w3"],
        "B_w2": _B_in["w2"],
    }

def reference(A_x, A_k_cache, A_v_cache, A_wq, A_wo, B_x, B_w1, B_w3, B_w2):
    def _A_ref(x, k_cache, v_cache, wq, wo):
        B, Sq, D = x.shape
        Hq, Dh = 32, 128
        Hkv, Sk = k_cache.shape[1], k_cache.shape[2]
        W = min(4096, max(256, Sk // 4))
        rep = Hq // Hkv
        q = (lambda x, *d: x.permute(*d))((x @ wq).reshape(B, Sq, Hq, Dh), 0, 2, 1, 3)
        k = (lambda x, n, axis: torch.repeat_interleave(x, n, dim=axis))(k_cache[:, :, Sk - W:, :], rep, axis=1)
        v = (lambda x, n, axis: torch.repeat_interleave(x, n, dim=axis))(v_cache[:, :, Sk - W:, :], rep, axis=1)
        scores = torch.einsum("bhqd,bhkd->bhqk", q, k) * (Dh ** -0.5)
        p = _softmax(scores, axis=-1)
        o = torch.einsum("bhqk,bhkd->bhqd", p, v)
        return (lambda x, *d: x.permute(*d))(o, 0, 2, 1, 3).reshape(B, Sq, Hq * Dh) @ wo

    def _B_ref(x, w1, w3, w2):
        gate = torch.einsum("bsd,df->bsf", x, w1)
        up = torch.einsum("bsd,df->bsf", x, w3)
        h = _silu(gate) * up
        out = torch.einsum("bsf,fd->bsd", h, w2)
        return out

    x = A_x
    h = x + _A_ref(_layernorm(x, axis=-1), A_k_cache, A_v_cache, A_wq, A_wo)
    h2 = h + _B_ref(_layernorm(h, axis=-1), B_w1, B_w3, B_w2)
    return h2
```

```cpp
#include <hip/hip_runtime.h>
#include <hip/hip_bf16.h>
#include <stdint.h>

typedef unsigned v4u __attribute__((ext_vector_type(4)));
typedef float f32x4 __attribute__((ext_vector_type(4)));
typedef short bf16x8 __attribute__((ext_vector_type(8)));
typedef unsigned short u16;

constexpr int D = 576, HQ = 32, HKV = 16, SK = 32768, DH = 128, W = 4096, F = 4608;
constexpr int NSPLIT = 8;

__device__ __forceinline__ float bf2f(unsigned b) { return __builtin_bit_cast(float, b << 16); }
__device__ __forceinline__ float bflo(unsigned w) { return __builtin_bit_cast(float, w << 16); }
__device__ __forceinline__ float bfhi(unsigned w) { return __builtin_bit_cast(float, w & 0xffff0000u); }
typedef __bf16 bf16x2_t __attribute__((ext_vector_type(2)));
typedef float f32x2 __attribute__((ext_vector_type(2)));
__device__ __forceinline__ unsigned cvtpk(float lo, float hi) { return __builtin_bit_cast(unsigned, __builtin_convertvector(f32x2{lo, hi}, bf16x2_t)); }
__device__ __forceinline__ f32x4 mfma(v4u a, v4u b, f32x4 c) {
  return __builtin_amdgcn_mfma_f32_16x16x32_bf16(__builtin_bit_cast(bf16x8, a), __builtin_bit_cast(bf16x8, b), c, 0, 0, 0);
}
template <int CTRL> __device__ __forceinline__ float dpp(float x) {
  return __builtin_bit_cast(float, __builtin_amdgcn_mov_dpp(__builtin_bit_cast(int, x), CTRL, 0xf, 0xf, true));
}
__device__ __forceinline__ float wave_sum(float v) {
  for (int o = 32; o; o >>= 1) v += __shfl_xor(v, o);
  return v;
}

__device__ void ln_lds(const float* src, float* dst, int tid) {
  int w = tid >> 6, lane = tid & 63;
  if (w < 2) {
    float v[9]; float s = 0;
    for (int i = 0; i < 9; ++i) { v[i] = src[w * D + lane + 64 * i]; s += v[i]; }
    s = wave_sum(s); float m = s * (1.f / D); float q = 0;
    for (int i = 0; i < 9; ++i) { float d = v[i] - m; q += d * d; }
    q = wave_sum(q); float r = 1.f / sqrtf(q * (1.f / D) + 1e-5f);
    for (int i = 0; i < 9; ++i) dst[w * D + lane + 64 * i] = bflo(cvtpk((v[i] - m) * r, 0.f));
  }
}

__global__ void __launch_bounds__(256) k_q(const u16* x, const u16* wq, u16* q) {
  __shared__ float xs[2 * D], xn[2 * D], red[256][17];
  int tid = threadIdx.x, c = blockIdx.x;
  for (int i = tid; i < 2 * D; i += 256) xs[i] = bf2f(x[i]);
  __syncthreads(); ln_lds(xs, xn, tid); __syncthreads();
  int half = tid & 1, rg = tid >> 1;
  float acc[2][8] = {};
  for (int r = rg; r < D; r += 128) {
    v4u wv = *(const v4u*)(wq + (size_t)r * 4096 + 16 * c + 8 * half);
    for (int b = 0; b < 2; ++b) { float a = xn[b * D + r];
      for (int k = 0; k < 4; ++k) { acc[b][2 * k] += a * bflo(wv[k]); acc[b][2 * k + 1] += a * bfhi(wv[k]); } }
  }
  for (int b = 0; b < 2; ++b) for (int k = 0; k < 8; ++k) red[tid][b * 8 + k] = acc[b][k];
  __syncthreads();
  if (tid < 32) { int b = tid >> 4, col = tid & 15, h = col >> 3, k = col & 7; float s = 0;
    for (int g = 0; g < 128; ++g) s += red[2 * g + h][b * 8 + k];
    q[b * 4096 + 16 * c + col] = (u16)cvtpk(s, 0.f); }
}

__global__ void __launch_bounds__(512) k_attn(const u16* q, const u16* kc, const u16* vc, float* part) {
  __shared__ float mrg[8][2][132];
  int tid = threadIdx.x, lane = tid & 63, w = tid >> 6;
  int c = blockIdx.x, s = c & 7, pair = c >> 3, b = pair >> 4, kvh = pair & 15;
  size_t base = ((size_t)(b * HKV + kvh) * SK + (SK - W) + s * 512 + w * 64) * DH;
  const u16* kp = kc + base + ((lane >> 2) & 3) * DH + (4 * (lane >> 4) + (lane & 3)) * 8;
  const u16* vp = vc + base + (lane >> 4) * DH + (lane & 15) * 8;
  v4u K[16], V[16];
#pragma unroll
  for (int t = 0; t < 16; ++t) K[t] = __builtin_nontemporal_load((const v4u*)(kp + t * 4 * DH));
#pragma unroll
  for (int t = 0; t < 16; ++t) V[t] = __builtin_nontemporal_load((const v4u*)(vp + t * 4 * DH));
  v4u qb = *(const v4u*)(q + b * 4096 + (kvh * 2 + ((lane >> 3) & 1)) * DH + (4 * (lane >> 4) + (lane & 3)) * 8);
  constexpr float C2 = 0.08838834764831845f * 1.4426950408889634f;
  float sc[16]; float mx = -INFINITY;
#pragma unroll
  for (int t = 0; t < 16; ++t) {
    f32x4 d = mfma(K[t], qb, f32x4{0, 0, 0, 0});
    float v = dpp<0x00>(d[0]) + dpp<0x55>(d[1]) + dpp<0xAA>(d[2]) + dpp<0xFF>(d[3]);
    sc[t] = v * C2; mx = fmaxf(mx, sc[t]);
  }
  mx = fmaxf(mx, __shfl_xor(mx, 16)); mx = fmaxf(mx, __shfl_xor(mx, 32));
  unsigned msk[4];
#pragma unroll
  for (int d = 0; d < 4; ++d) msk[d] = (((lane & 7) >> 1) == d) ? ((lane & 1) ? 0xffff0000u : 0x0000ffffu) : 0u;
  f32x4 acc = {0, 0, 0, 0}; float l = 0;
#pragma unroll
  for (int t = 0; t < 16; ++t) {
    float p = __builtin_amdgcn_exp2f(sc[t] - mx); l += p;
    unsigned pp = cvtpk(p, p);
    v4u a = {pp & msk[0], pp & msk[1], pp & msk[2], pp & msk[3]};
    acc = mfma(a, V[t], acc);
  }
  l += __shfl_xor(l, 16); l += __shfl_xor(l, 32);
  *(f32x4*)&mrg[w][lane >> 5][4 + 8 * (lane & 15) + 4 * ((lane >> 4) & 1)] = acc;
  if (lane == 0 || lane == 8) { mrg[w][lane >> 3][0] = mx; mrg[w][lane >> 3][1] = l; }
  __syncthreads();
  if (tid < 256) { int h = tid >> 7, d = tid & 127; float M = -INFINITY;
    for (int i = 0; i < 8; ++i) M = fmaxf(M, mrg[i][h][0]);
    float L = 0, o = 0;
    for (int i = 0; i < 8; ++i) { float e = __builtin_amdgcn_exp2f(mrg[i][h][0] - M); L += e * mrg[i][h][1]; o += e * mrg[i][h][4 + d]; }
    float* rec = part + ((size_t)(b * HQ + kvh * 2 + h) * NSPLIT + s) * 132;
    rec[4 + d] = o; if (d == 0) { rec[0] = M; rec[1] = L; } }
}

__global__ void __launch_bounds__(256) k_wo(const float* part, const u16* wo, float* p1) {
  __shared__ float os[2][16];
  int tid = threadIdx.x, c = blockIdx.x, h = c >> 3, d0 = (c & 7) * 16;
  if (tid < 32) { int b = tid >> 4, d = tid & 15; const float* rec = part + (size_t)(b * HQ + h) * NSPLIT * 132;
    float M = -INFINITY; for (int s = 0; s < NSPLIT; ++s) M = fmaxf(M, rec[s * 132]);
    float L = 0, o = 0; for (int s = 0; s < NSPLIT; ++s) { float e = __builtin_amdgcn_exp2f(rec[s * 132] - M); L += e * rec[s * 132 + 1]; o += e * rec[s * 132 + 4 + d0 + d]; }
    os[b][d] = bflo(cvtpk(o / L, 0.f)); }
  __syncthreads();
  for (int col = tid; col < D; col += 256) { float a0 = 0, a1 = 0;
    for (int r = 0; r < 16; ++r) { float wv = bf2f(wo[(size_t)(16 * c + r) * D + col]); a0 += os[0][r] * wv; a1 += os[1][r] * wv; }
    p1[c * 1152 + col] = a0; p1[c * 1152 + D + col] = a1; }
}
__global__ void __launch_bounds__(256) k_red(const float* p, int n, const u16* resb, const float* resf, float* outf, u16* outb) {
  __shared__ float red[16][17];
  int tid = threadIdx.x, i = blockIdx.x * 16 + (tid & 15), g = tid >> 4; float s = 0;
  for (int c = g; c < n; c += 16) s += p[c * 1152 + i];
  red[g][tid & 15] = s; __syncthreads();
  if (tid < 16) { float t = 0; for (int k = 0; k < 16; ++k) t += red[k][tid];
    t += resb ? bf2f(resb[i]) : resf[i];
    if (outf) outf[i] = t; if (outb) outb[i] = (u16)cvtpk(t, 0.f); }
}
__global__ void __launch_bounds__(256) k_ffn(const float* h, const u16* w1, const u16* w3, const u16* w2, float* p2) {
  __shared__ float xs[2 * D], xn[2 * D], red[256][33], ss[2][32];
  int tid = threadIdx.x, c = blockIdx.x;
  for (int i = tid; i < 2 * D; i += 256) xs[i] = h[i];
  __syncthreads(); ln_lds(xs, xn, tid); __syncthreads();
  int qt = tid & 3, rg = tid >> 2;
  float acc[2][2][8] = {};
  for (int r = rg; r < D; r += 64) {
    v4u a = *(const v4u*)(w1 + (size_t)r * F + 32 * c + 8 * qt), u = *(const v4u*)(w3 + (size_t)r * F + 32 * c + 8 * qt);
    for (int b = 0; b < 2; ++b) { float xv = xn[b * D + r];
      for (int k = 0; k < 4; ++k) { acc[0][b][2 * k] += xv * bflo(a[k]); acc[0][b][2 * k + 1] += xv * bfhi(a[k]); acc[1][b][2 * k] += xv * bflo(u[k]); acc[1][b][2 * k + 1] += xv * bfhi(u[k]); } }
  }
  for (int m = 0; m < 2; ++m) for (int b = 0; b < 2; ++b) for (int k = 0; k < 8; ++k) red[tid][(m * 2 + b) * 8 + k] = acc[m][b][k];
  __syncthreads();
  if (tid < 64) { int b = tid >> 5, col = tid & 31; float g = 0, u = 0;
    for (int r = 0; r < 64; ++r) { g += red[4 * r + (col >> 3)][b * 8 + (col & 7)]; u += red[4 * r + (col >> 3)][(2 + b) * 8 + (col & 7)]; }
    ss[b][col] = bflo(cvtpk(g / (1.f + __expf(-g)) * u, 0.f)); }
  __syncthreads();
  for (int col = tid; col < D; col += 256) { float a0 = 0, a1 = 0;
    for (int r = 0; r < 32; ++r) { float wv = bf2f(w2[(size_t)(32 * c + r) * D + col]); a0 += ss[0][r] * wv; a1 += ss[1][r] * wv; }
    p2[c * 1152 + col] = a0; p2[c * 1152 + D + col] = a1; }
}

extern "C" void kernel_launch(void* const* d_in, const int* in_sizes, int n_in, void* d_out, int out_size, void* d_ws, size_t ws_size, hipStream_t stream) {
  const u16* x = (const u16*)d_in[0]; const u16* kc = (const u16*)d_in[1]; const u16* vc = (const u16*)d_in[2];
  const u16* wq = (const u16*)d_in[3]; const u16* wo = (const u16*)d_in[4];
  const u16* w1 = (const u16*)d_in[6]; const u16* w3 = (const u16*)d_in[7]; const u16* w2 = (const u16*)d_in[8];
  char* ws = (char*)d_ws;
  u16* q = (u16*)ws; float* part = (float*)(ws + 65536); float* p1 = (float*)(ws + (1 << 20)); float* h = (float*)(ws + (3 << 20)); float* p2 = (float*)(ws + (4 << 20));
  k_q<<<256, 256, 0, stream>>>(x, wq, q);
  k_attn<<<256, 512, 0, stream>>>(q, kc, vc, part);
  k_wo<<<256, 256, 0, stream>>>(part, wo, p1);
  k_red<<<72, 256, 0, stream>>>(p1, 256, x, nullptr, h, nullptr);
  k_ffn<<<144, 256, 0, stream>>>(h, w1, w3, w2, p2);
  k_red<<<72, 256, 0, stream>>>(p2, 144, nullptr, h, nullptr, (u16*)d_out);
}
```

```cpp
#include <hip/hip_runtime.h>
#include <hip/hip_bf16.h>
#include <stdint.h>

typedef unsigned v2u __attribute__((ext_vector_type(2)));
typedef unsigned v4u __attribute__((ext_vector_type(4)));
typedef unsigned v8u __attribute__((ext_vector_type(8)));
typedef float f32x2 __attribute__((ext_vector_type(2)));
typedef float f32x4 __attribute__((ext_vector_type(4)));
typedef short s16x4 __attribute__((ext_vector_type(4)));
typedef short s16x8 __attribute__((ext_vector_type(8)));
typedef __bf16 bf16x2_t __attribute__((ext_vector_type(2)));
typedef __bf16 bf16x8_t __attribute__((ext_vector_type(8)));
typedef __bf16 bf16x16_t __attribute__((ext_vector_type(16)));
typedef unsigned short u16;
typedef __amdgpu_buffer_rsrc_t rsrc_t;
#define LDS __attribute__((address_space(3)))
#define GLB __attribute__((address_space(1)))
#define DEV __device__ __forceinline__
#define PIN() __builtin_amdgcn_sched_barrier(0)

constexpr int D = 576, SK = 32768, WIN = 4096, FF = 4608;
constexpr int NLF = 17, NLO = 13;
constexpr int AUX_NT = 2, AUX_SC1 = 16;
#define ROT_C 5
#define ROT_W 15
constexpr unsigned WS_QP = 4096;
constexpr unsigned WS_AP = WS_QP + 256 * 2048;
constexpr unsigned WS_OP = WS_AP + 256 * 1152;
constexpr unsigned WS_HB = WS_OP + 64 * 4608;
constexpr unsigned WS_DP = WS_HB + 4608;
constexpr unsigned WS_END = WS_DP + 144 * 4608;
constexpr unsigned L_CNTI = 0, L_QF = 4, L_CNTM = 8, L_CNTF = 12, L_CNTW = 16, L_CNTG = 20, L_OF = 24;
constexpr unsigned L_TAG = 32;
constexpr unsigned L_ST = 48;
constexpr unsigned L_MX = 64;
constexpr unsigned L_L1 = 128;
constexpr unsigned L_Q = 192;
constexpr unsigned L_P1 = 704;
constexpr unsigned L_GP = L_P1 + 8192;
constexpr unsigned L_CS = L_GP + 2048;
constexpr unsigned GH = 2336;
constexpr unsigned L_GI = L_CS + 1024;
constexpr unsigned OH = 1024 + 64, OB = 512 + 32;
constexpr unsigned SH = 512 + 64, SB = 256 + 32;
constexpr unsigned L_DI = L_GI + 4 * 2 * GH;
constexpr unsigned SI = 2 * SH + 64;
constexpr unsigned L_END = L_DI + 5 * SI;
constexpr unsigned SPIN_G = 1u << 18, SPIN_L = 1u << 21;

#define ST_DECL
#define ST(i)
#define ST_PARAM
#define ST_ARG

DEV float asf(unsigned u) { return __builtin_bit_cast(float, u); }
DEV unsigned asu(float f) { return __builtin_bit_cast(unsigned, f); }
DEV float bflo(unsigned w) { return asf(w << 16); }
DEV float bfhi(unsigned w) { return asf(w & 0xffff0000u); }
DEV unsigned cvtpk(float lo, float hi) { return __builtin_bit_cast(unsigned, __builtin_convertvector(f32x2{lo, hi}, bf16x2_t)); }
DEV unsigned hilo(float v) { float h = asf(asu(v) & 0xffff0000u); return cvtpk(h, v - h); }
DEV f32x4 mfma32(v4u a, v4u b, f32x4 c) {
  return __builtin_amdgcn_mfma_f32_16x16x32_bf16(__builtin_bit_cast(bf16x8_t, a), __builtin_bit_cast(bf16x8_t, b), c, 0, 0, 0);
}
DEV f32x4 sm32(v2u a, v4u b, f32x4 c, unsigned idx) {
  return __builtin_amdgcn_smfmac_f32_16x16x32_bf16(__builtin_bit_cast(s16x4, a), __builtin_bit_cast(s16x8, b), c, idx, 0, 0);
}
DEV f32x4 sm64(v4u a, v4u b0, v4u b1, f32x4 c, unsigned idx) {
  v8u b = __builtin_shufflevector(b0, b1, 0, 1, 2, 3, 4, 5, 6, 7);
  return __builtin_amdgcn_smfmac_f32_16x16x64_bf16(__builtin_bit_cast(bf16x8_t, a), __builtin_bit_cast(bf16x16_t, b), c, idx, 0, 0);
}
template <int CTRL> DEV float dpp(float x) { return asf(__builtin_amdgcn_mov_dpp(asu(x), CTRL, 0xf, 0xf, true)); }
DEV float sw32add(float a, float b) { auto r = __builtin_amdgcn_permlane32_swap(asu(a), asu(b), false, false); return asf(r[0]) + asf(r[1]); }
DEV float sw16add(float a, float b) { auto r = __builtin_amdgcn_permlane16_swap(asu(a), asu(b), false, false); return asf(r[0]) + asf(r[1]); }
DEV float sw32max(float a) { auto r = __builtin_amdgcn_permlane32_swap(asu(a), asu(a), false, false); return fmaxf(asf(r[0]), asf(r[1])); }
DEV float sw16max(float a) { auto r = __builtin_amdgcn_permlane16_swap(asu(a), asu(a), false, false); return fmaxf(asf(r[0]), asf(r[1])); }
DEV float sum32(float v) {
  v += dpp<0xB1>(v); v += dpp<0x4E>(v); v += dpp<0x124>(v); v += dpp<0x128>(v); return sw16add(v, v);
}
DEV rsrc_t mkrs(const void* p, unsigned n) { return __builtin_amdgcn_make_buffer_rsrc((void*)p, 0, n, 0x00020000); }
template <int AUX> DEV v4u ld16(rsrc_t r, unsigned vo, unsigned so) { return __builtin_amdgcn_raw_buffer_load_b128(r, vo, so, AUX); }

struct Tag { unsigned t[4]; };
DEV unsigned tagw(float v, unsigned t) { return (asu(v) & 0xfffff800u) | (t | 0x400u); }
DEV v4u pack(f32x4 v, const Tag& g) { return v4u{tagw(v[0], g.t[0]), tagw(v[1], g.t[1]), tagw(v[2], g.t[2]), tagw(v[3], g.t[3])}; }
DEV void publish(rsrc_t ws, unsigned vo, unsigned so, v4u d) { __builtin_amdgcn_raw_buffer_store_b128(d, ws, vo, so, AUX_SC1); }
template <int N, typename FO> DEV void sweep(rsrc_t ws, v4u (&u)[N], const Tag& g, FO off) {
#pragma unroll 1
  for (unsigned spin = 0; spin < SPIN_G; ++spin) {
    unsigned bad = 0;
#pragma unroll
    for (int i = 0; i < N; ++i) { unsigned vo, so; off(i, vo, so); u[i] = ld16<AUX_SC1>(ws, vo, so); }
    PIN();
#pragma unroll
    for (int i = 0; i < N; ++i) { u[i] ^= v4u{g.t[0], g.t[1], g.t[2], g.t[3]}; bad |= u[i][0] | u[i][1] | u[i][2] | u[i][3]; }
    if (!__builtin_amdgcn_ballot_w64((bad & 0x3ffu) != 0)) break;
  }
}
DEV f32x4 asf4(v4u u) { return __builtin_bit_cast(f32x4, u); }

template <typename T> DEV LDS T* lp(unsigned off) { return (LDS T*)(uintptr_t)off; }
DEV void lds_inc(unsigned off) { asm volatile("s_mov_b64 exec, 1\n\tds_add_u32 %0, %1\n\ts_mov_b64 exec, -1" ::"v"(off), "v"(1u) : "memory"); }
DEV unsigned lds_inc_rtn(unsigned off) { unsigned o;
  asm volatile("s_mov_b64 exec, 1\n\tds_add_rtn_u32 %0, %1, %2\n\ts_mov_b64 exec, -1\n\ts_waitcnt lgkmcnt(0)" : "=v"(o) : "v"(off), "v"(1u) : "memory");
  return __builtin_amdgcn_readfirstlane(o); }
template <int SLEEP> DEV void lds_spin(unsigned off, unsigned val) { unsigned v, n;
  asm volatile("s_mov_b32 %1, %4\n"
               ".Lspin%=:\n\tds_read_b32 %0, %2\n\ts_sub_u32 %1, %1, 1\n\ts_waitcnt lgkmcnt(0)\n\ts_cbranch_scc1 .Ldone%=\n\t"
               "v_cmp_gt_u32 vcc, %3, %0\n\ts_cbranch_vccz .Ldone%=\n\ts_sleep %5\n\ts_branch .Lspin%=\n"
               ".Ldone%=:" : "=&v"(v), "=&s"(n) : "v"(off), "s"(val), "n"(SPIN_L), "n"(SLEEP) : "memory", "vcc", "scc"); }
DEV void lds_wait(unsigned off, unsigned val) { lds_spin<0>(off, val); }
DEV void lds_wait_slow(unsigned off, unsigned val) { lds_spin<2>(off, val); }
DEV Tag lds_tag() { v4u t = *lp<v4u>(L_TAG); Tag g;
#pragma unroll
  for (int i = 0; i < 4; ++i) g.t[i] = __builtin_amdgcn_readfirstlane(t[i]);
  return g; }

template <int NU> DEV void down_load(v4u (&w)[2 * NU], rsrc_t rw, unsigned base, int cb, int lane) {
  const int gb = lane >> 4;
  const unsigned vo = base + (2 * NU * (gb >> 1) + (gb & 1)) * (D * 2) + cb * 256 + (lane & 15) * 16;
#pragma unroll
  for (int i = 0; i < 2 * NU; ++i) w[i] = ld16<AUX_NT>(rw, vo, (4 * NU * (i & 1) + 2 * (i >> 1)) * (D * 2));
  PIN();
}
template <int NU, unsigned HS, unsigned BS> DEV void down_full(const v4u (&w)[2 * NU], unsigned img, rsrc_t ws, unsigned dst, int cb, int lane, const Tag& g) {
  const unsigned ia = img + ((lane >> 2) & 1) * HS + ((lane >> 3) & 1) * BS + (lane >> 4) * NU * 16, idx = (lane & 3) * 0x5555u;
  f32x4 acc = {0, 0, 0, 0};
#pragma unroll
  for (int u = 0; u < NU; ++u) acc = sm64(*lp<v4u>(ia + u * 16), w[2 * u], w[2 * u + 1], acc, idx);
  publish(ws, ((lane >> 5) * 144 + 2 * (lane & 15) + ((lane >> 4) & 1)) * 16, dst + cb * 512, pack(acc, g));
}
template <int NU> DEV void tail_load(v4u (&w)[NU], rsrc_t rw, unsigned base, int lane) {
  const int gb = lane >> 4;
  const unsigned vo = base + (NU * (2 * (gb >> 1) + ((lane >> 3) & 1)) + (gb & 1)) * (D * 2) + 1024 + (lane & 7) * 16;
#pragma unroll
  for (int i = 0; i < NU; ++i) w[i] = ld16<AUX_NT>(rw, vo, (4 * NU * (i & 1) + 2 * (i >> 1)) * (D * 2));
  PIN();
}
template <int NU, unsigned HS, unsigned BS> DEV void down_tail(const v4u (&w)[NU], unsigned img, rsrc_t ws, unsigned dst, int lane, const Tag& g) {
  const unsigned ia = img + ((lane >> 2) & 1) * HS + (2 * (lane >> 4) + ((lane >> 3) & 1)) * (NU / 2) * 16, idx = (lane & 3) * 0x5555u;
  f32x4 a0 = {0, 0, 0, 0}, a1 = {0, 0, 0, 0};
#pragma unroll
  for (int u = 0; u < NU / 2; ++u) { a0 = sm64(*lp<v4u>(ia + u * 16), w[2 * u], w[2 * u + 1], a0, idx); a1 = sm64(*lp<v4u>(ia + BS + u * 16), w[2 * u], w[2 * u + 1], a1, idx); }
  f32x4 r;
#pragma unroll
  for (int k = 0; k < 4; ++k) { auto s = __builtin_amdgcn_permlane32_swap(asu(a0[k]), asu(a1[k]), false, false); r[k] = asf(s[0]) + dpp<0x128>(asf(s[1])); }
  if (!(lane & 8)) publish(ws, ((lane >> 5) * 144 + 128 + 2 * (lane & 7) + ((lane >> 4) & 1)) * 16, dst, pack(r, g));
}
DEV void lds_zero(unsigned off, int n16, int lane) { for (int i = lane; i < n16; i += 64) *lp<v4u>(off + i * 16) = v4u{0, 0, 0, 0}; }

DEV void silu_stage(unsigned img, int lane) {
  lds_wait(L_CNTG, 5);
  const int b = lane >> 5, un = lane & 31;
  float g = 0, u = 0, cg = 0, cu = 0;
#pragma unroll
  for (int a = 0; a < 4; ++a) { g += *lp<float>(L_GP + a * 512 + b * 256 + un * 4); u += *lp<float>(L_GP + a * 512 + b * 256 + 128 + un * 4);
    cg += *lp<float>(L_CS + a * 256 + un * 4); cu += *lp<float>(L_CS + a * 256 + 128 + un * 4); }
  f32x2 st = *lp<f32x2>(L_ST + b * 8);
  g = (g - st[0] * cg) * st[1]; u = (u - st[0] * cu) * st[1];
  float s = g * u * __builtin_amdgcn_rcpf(1.f + __builtin_amdgcn_exp2f(g * -1.4426950408889634f));
  unsigned c = hilo(s), ad = img + b * SB + un * 8;
  *lp<unsigned>(ad) = c; *lp<unsigned>(ad + SH + 4) = c;
}

template <int NL> DEV void attention(const u16* kc, const u16* vc, rsrc_t ws, unsigned base, int rot, int sw, int cu, int lane, Tag& g ST_PARAM) {
  const rsrc_t rk = mkrs(kc, 2u * 16 * SK * 256), rv = mkrs(vc, 2u * 16 * SK * 256);
  const unsigned vk = base + ((lane >> 2) & 3) * 256 + (4 * (lane >> 4) + (lane & 3)) * 16, vv = base + lane * 16;
  v4u K[NL], V[NL]; unsigned so[NL];
  { unsigned s = rot * 1024;
#pragma unroll
    for (int t = 0; t < NL; ++t) { so[t] = s; s += 1024; if (s == NL * 1024) s = 0; } }
#pragma unroll
  for (int t = 0; t < 4; ++t) K[t] = ld16<AUX_NT>(rk, vk, so[t]);
  PIN(); asm volatile("s_barrier" ::: "memory"); PIN();
#pragma unroll
  for (int t = 4; t < NL; ++t) K[t] = ld16<AUX_NT>(rk, vk, so[t]);
  PIN();
#pragma unroll
  for (int t = 0; t < NL; ++t) V[t] = ld16<AUX_NT>(rv, vv, so[t]);
  PIN();
  lds_inc(L_CNTI);
  ST(1);
  lds_wait_slow(L_QF, 1);
  ST(2);
  g = lds_tag();
  const v4u qb = *lp<v4u>(L_Q + ((lane >> 3) & 1) * 256 + (4 * (lane >> 4) + (lane & 3)) * 16);
  constexpr float C2 = 0.08838834764831845f * 1.4426950408889634f;
  float sc[NL], mx = -INFINITY;
#pragma unroll
  for (int t = 0; t < NL; ++t) {
    f32x4 d = mfma32(K[t], qb, f32x4{0, 0, 0, 0});
    sc[t] = (dpp<0x00>(d[0]) + dpp<0x55>(d[1]) + dpp<0xAA>(d[2]) + dpp<0xFF>(d[3])) * C2;
    mx = fmaxf(mx, sc[t]);
  }
  mx = sw32max(sw16max(mx));
  const int hs = (lane >> 3) & 1, ha = lane >> 5;
  *lp<float>(L_MX + sw * 8 + hs * 4) = mx;
  f32x4 acc = {0, 0, 0, 0}; float l = 0;
  ST(3);
  if (sw >= 4) {
    lds_inc(L_CNTM);
    lds_wait(L_CNTM, 8);
    float ma = -INFINITY;
#pragma unroll
    for (int i = 0; i < 8; ++i) { mx = fmaxf(mx, *lp<float>(L_MX + i * 8 + hs * 4)); ma = fmaxf(ma, *lp<float>(L_MX + i * 8 + ha * 4)); }
    acc = *lp<f32x4>(L_P1 + (sw - 4) * 1024 + lane * 16) * __builtin_amdgcn_exp2f(*lp<float>(L_MX + (sw - 4) * 8 + ha * 4) - ma);
    if (lane < 16) l = *lp<float>(L_L1 + (sw - 4) * 8 + hs * 4) * __builtin_amdgcn_exp2f(*lp<float>(L_MX + (sw - 4) * 8 + hs * 4) - mx);
  }
  const unsigned mA = (lane & 4) ? 0u : ~0u, idx = (lane & 3) * 0x5555u;
  v2u A[NL];
#pragma unroll
  for (int t = 0; t < NL; ++t) { float p = __builtin_amdgcn_exp2f(sc[t] - mx); l += p; unsigned c = hilo(p); A[t] = v2u{c & mA, c & ~mA}; }
  l = sw16add(l, l); l = sw32add(l, l);
  *lp<float>(L_L1 + sw * 8 + hs * 4) = l;
  __builtin_amdgcn_sched_barrier(0);
#pragma unroll
  for (int t = 0; t < NL; ++t) acc = sm32(A[t], V[t], acc, idx);
  *lp<f32x4>(L_P1 + sw * 1024 + lane * 16) = acc;
  ST(4);
  if (sw < 4) { lds_inc(L_CNTM); return; }
  if (lds_inc_rtn(L_CNTF) == 3) {
    __builtin_amdgcn_s_setprio(3);
    f32x4 o = *lp<f32x4>(L_P1 + 4096 + lane * 16); float L = *lp<float>(L_L1 + 32 + hs * 4);
#pragma unroll
    for (int i = 1; i < 4; ++i) { o += *lp<f32x4>(L_P1 + 4096 + i * 1024 + lane * 16); L += *lp<float>(L_L1 + 32 + i * 8 + hs * 4); }
    publish(ws, (ha * 32 + 2 * (lane & 15) + ((lane >> 4) & 1)) * 16, WS_AP + cu * 1152, pack(o, g));
    v4u ml = pack(f32x4{mx, asf(asu(mx) << 21), L, L}, g);
    if (lane < 16 && !(lane & 4)) publish(ws, 1024 + (hs * 4 + (lane & 3)) * 16, WS_AP + cu * 1152, ml);
    __builtin_amdgcn_s_setprio(0);
  }
  ST(5);
}

__global__ void __launch_bounds__(768, 3)
fused_decode_kernel(const u16* __restrict__ x, const u16* __restrict__ kc, const u16* __restrict__ vc, const u16* __restrict__ wq, const u16* __restrict__ wo,
                    const u16* __restrict__ w1, const u16* __restrict__ w3, const u16* __restrict__ w2, u16* __restrict__ out, unsigned* __restrict__ wsp) {
  __shared__ v4u lds_all[L_END / 16 + 1];
  asm volatile("" ::"s"((LDS v4u*)lds_all), "s"(x), "s"(kc), "s"(vc), "s"(wq), "s"(wo), "s"(w1), "s"(w3), "s"(w2), "s"(out), "s"(wsp));
  ST_DECL; ST(0);
  const int lane = threadIdx.x & 63, w = __builtin_amdgcn_readfirstlane(threadIdx.x >> 6), c = blockIdx.x;
  const int kvh = 2 * (c & 7) + (c >> 7), j = (c >> 3) & 15, b = j >> 3, s = j & 7, cu = kvh * 16 + j;
  const bool isO = s < 2, isF = !isO && s < 7 - b;
  const int oi = kvh * 4 + b * 2 + s, fi = kvh * 9 + b * 5 + s - 2, ri = kvh * 3 + (b ? s - 5 : 0);
  const rsrc_t ws = mkrs(wsp, WS_END);
  Tag g;
  if (w >= 4) {
    const int sw = w - 4;
    const unsigned row0 = (unsigned)(b * 16 + kvh) * SK + (SK - WIN);
    if (isO) attention<NLO>(kc, vc, ws, (row0 + s * (32 * NLO) + sw * (4 * NLO)) * 256, (c * ROT_C + sw * ROT_W) % NLO, sw, cu, lane, g ST_ARG);
    else attention<NLF>(kc, vc, ws, (row0 + 64 * NLO + (s - 2) * (32 * NLF) + sw * (4 * NLF)) * 256, (c * ROT_C + sw * ROT_W) % NLF, sw, cu, lane, g ST_ARG);
    if (isO) {
      if (sw == 0) {
        v4u wt[8]; lds_wait_slow(L_CNTI, 8); tail_load<8>(wt, mkrs(wo, 4096 * D * 2), oi * 64 * (D * 2), lane);
        lds_wait(L_OF, 1); ST(7);
        down_tail<8, OH, OB>(wt, L_DI, ws, WS_OP + oi * 4608, lane, g); ST(8);
      } else if (sw == 4) {
        lds_zero(L_DI, (2 * OH) / 16, lane);
        lds_wait_slow(L_CNTW, 4); ST(6);
        const int jo = oi & 3; v4u u[8];
        const unsigned vo = (lane >> 4) * 1152 + (jo * 16 + (lane & 15)) * 16, vm = (lane >> 4) * 1152 + 1024 + (jo >> 1) * 64;
        sweep<8>(ws, u, g, [&](int i, unsigned& v, unsigned& so) { v = i < 4 ? vo : vm; so = WS_AP + (kvh * 16 + (i & 3) * 4) * 1152; });
        ST(7);
        f32x4 ob[2]; float Lb[2];
#pragma unroll
        for (int bb = 0; bb < 2; ++bb) {
          float m0 = asf((u[4 + 2 * bb][0] & 0xfffff800u) | (u[4 + 2 * bb][1] >> 21)), m1 = asf((u[5 + 2 * bb][0] & 0xfffff800u) | (u[5 + 2 * bb][1] >> 21));
          float M = sw32max(sw16max(fmaxf(m0, m1))), e0 = __builtin_amdgcn_exp2f(m0 - M), e1 = __builtin_amdgcn_exp2f(m1 - M);
          ob[bb] = asf4(u[2 * bb]) * e0 + asf4(u[2 * bb + 1]) * e1; Lb[bb] = asf(u[4 + 2 * bb][2]) * e0 + asf(u[5 + 2 * bb][2]) * e1;
        }
        float L = sw32add(Lb[0], Lb[1]); L = sw16add(L, L);
        float r0 = sw32add(ob[0][0], ob[1][0]), r1 = sw32add(ob[0][1], ob[1][1]), r2 = sw32add(ob[0][2], ob[1][2]), r3 = sw32add(ob[0][3], ob[1][3]);
        float inv = __builtin_amdgcn_rcpf(L);
        unsigned c0 = hilo(sw16add(r0, r1) * inv), c1 = hilo(sw16add(r2, r3) * inv);
        unsigned ad = L_DI + (lane >> 5) * OB + (lane & 15) * 32 + ((lane >> 4) & 1) * 8;
        *lp<unsigned>(ad) = c0; *lp<unsigned>(ad + 16) = c1; *lp<unsigned>(ad + OH + 4) = c0; *lp<unsigned>(ad + OH + 20) = c1;
        lds_inc(L_OF); ST(8);
      }
    } else if (isF) {
      if (sw < 4) {
        v4u wd[8]; lds_zero(L_DI + sw * SI, SI / 16, lane);
        lds_wait_slow(L_CNTI, 8); down_load<4>(wd, mkrs(w2, FF * D * 2), fi * 32 * (D * 2), sw, lane);
        ST(6);
        silu_stage(L_DI + sw * SI, lane); ST(7);
        down_full<4, SH, SB>(wd, L_DI + sw * SI, ws, WS_DP + fi * 4608, sw, lane, g); ST(8);
      } else if (sw == 5) {
        v4u wt[4]; tail_load<4>(wt, mkrs(w2, FF * D * 2), fi * 32 * (D * 2), lane); lds_zero(L_DI + 4 * SI, SI / 16, lane);
        silu_stage(L_DI + 4 * SI, lane); ST(7);
        down_tail<4, SH, SB>(wt, L_DI + 4 * SI, ws, WS_DP + fi * 4608, lane, g); ST(8);
      } else if (sw == 4) {
        lds_wait_slow(L_CNTW, 4); ST(6);
        v4u u[5]; const unsigned vo = ((lane >> 5) * 144 + (lane & 31)) * 16, vt = ((lane >> 5) * 144 + (lane & 15)) * 16;
        sweep<5>(ws, u, g, [&](int i, unsigned& v, unsigned& so) { v = i < 4 ? vo : vt; so = WS_HB + i * 512; });
        ST(7);
        const float wl = (lane & 16) ? 0.f : 1.f;
        f32x4 sv = asf4(u[0]) + asf4(u[1]) + asf4(u[2]) + asf4(u[3]) + asf4(u[4]) * wl;
        const float m = sum32(sv[0] + sv[1] + sv[2] + sv[3]) * (1.f / D);
        f32x4 q = {0, 0, 0, 0};
#pragma unroll
        for (int i = 0; i < 4; ++i) { f32x4 d = asf4(u[i]) - m; q += d * d; }
        { f32x4 d = asf4(u[4]) - m; q += d * d * wl; }
        const float var = sum32(q[0] + q[1] + q[2] + q[3]) * (1.f / D);
        *lp<f32x2>(L_ST + (lane >> 5) * 8) = f32x2{m, __builtin_amdgcn_rsqf(var + 1e-5f)};
        lds_inc(L_CNTG); ST(8);
      }
    } else if (sw == 4 && ri < 36) {
      const int rg = (lane >> 5) + 2 * ((lane >> 4) & 1), col = ri * 32 + (lane & 7) * 4 + rg;
      const unsigned tg = rg == 0 ? g.t[0] : rg == 1 ? g.t[1] : rg == 2 ? g.t[2] : g.t[3];
      const float xv = bflo(x[col]);
      const unsigned vo = (lane >> 3) * 4608 + (ri * 8 + (lane & 7)) * 16;
      float h;
      { v4u u[8]; sweep<8>(ws, u, g, [&](int i, unsigned& v, unsigned& so) { v = vo; so = WS_OP + i * 8 * 4608; });
        ST(6);
        f32x4 a = asf4(u[0]);
#pragma unroll
        for (int i = 1; i < 8; ++i) a += asf4(u[i]);
        h = sw16add(sw32add(a[0], a[1]), sw32add(a[2], a[3])); h += dpp<0x128>(h); h += xv;
        if (!(lane & 8)) __builtin_amdgcn_raw_buffer_store_b32(tagw(h, tg), ws, col * 4, WS_HB, AUX_SC1);
        ST(7); }
      { v4u u[18]; sweep<18>(ws, u, g, [&](int i, unsigned& v, unsigned& so) { v = vo; so = WS_DP + i * 8 * 4608; });
        ST(8);
        f32x4 a = asf4(u[0]);
#pragma unroll
        for (int i = 1; i < 18; ++i) a += asf4(u[i]);
        float y = sw16add(sw32add(a[0], a[1]), sw32add(a[2], a[3])); y += dpp<0x128>(y); y += h;
        if (!(lane & 8)) out[col] = (u16)cvtpk(y, y);
        if (ri == 0 && lane == 0) publish(ws, 0, 0, v4u{g.t[0], g.t[1], g.t[2], g.t[3]});
      }
    }
  } else {
    if (w == 1) { if (lane < 8) *lp<unsigned>(lane * 4) = 0; asm volatile("s_waitcnt lgkmcnt(0)" ::: "memory"); }
    if (w == 0) {
      __builtin_amdgcn_s_setprio(3);
      const rsrc_t rx = mkrs(x, 2 * D * 2), rq = mkrs(wq, D * 4096 * 2);
      v4u ep = ld16<AUX_SC1>(ws, 0, 0);
      unsigned xs[9], xa[9]; v4u wv[18];
#pragma unroll
      for (int k = 0; k < 9; ++k) xs[k] = __builtin_amdgcn_raw_buffer_load_b32(rx, (lane >> 5) * (D * 2) + (lane & 31) * 4, k * 128, 0);
#pragma unroll
      for (int t = 0; t < 9; ++t) xa[t] = __builtin_amdgcn_raw_buffer_load_b16(rx, (((lane >> 3) & 1) * D + 36 * j + (lane >> 4)) * 2, t * 8, 0);
      PIN();
      const unsigned vq = ((36 * j + (lane >> 4)) * 4096 + kvh * 256 + (lane & 15) * 8) * 2;
#pragma unroll
      for (int i = 0; i < 18; ++i) wv[i] = ld16<AUX_NT>(rq, vq, (i >> 1) * 4 * 8192 + (i & 1) * 256);
      PIN(); asm volatile("s_barrier" ::: "memory"); PIN();
#pragma unroll
      for (int i = 0; i < 4; ++i) g.t[i] = (__builtin_amdgcn_readfirstlane(ep[i]) + 1) & 0x3ffu;
      float sm = 0;
#pragma unroll
      for (int k = 0; k < 9; ++k) sm += bflo(xs[k]) + bfhi(xs[k]);
      const float m = sum32(sm) * (1.f / D); float q = 0;
#pragma unroll
      for (int k = 0; k < 9; ++k) { float d0 = bflo(xs[k]) - m, d1 = bfhi(xs[k]) - m; q += d0 * d0 + d1 * d1; }
      const float r = __builtin_amdgcn_rsqf(sum32(q) * (1.f / D) + 1e-5f);
      const float m0 = asf(__builtin_amdgcn_readlane(asu(m), 0)), m1 = asf(__builtin_amdgcn_readlane(asu(m), 32));
      const float r0 = asf(__builtin_amdgcn_readlane(asu(r), 0)), r1 = asf(__builtin_amdgcn_readlane(asu(r), 32));
      const float mm = (lane & 8) ? m1 : m0, rr = (lane & 8) ? r1 : r0;
      const unsigned mA = (lane & 4) ? 0u : ~0u, idx = (lane & 3) * 0x5555u;
      f32x4 a0 = {0, 0, 0, 0}, a1 = {0, 0, 0, 0};
#pragma unroll
      for (int t = 0; t < 9; ++t) { unsigned cc = hilo((bflo(xa[t]) - mm) * rr); v2u A = {cc & mA, cc & ~mA};
        a0 = sm32(A, wv[2 * t], a0, idx); a1 = sm32(A, wv[2 * t + 1], a1, idx); }
      const unsigned vp = ((lane >> 5) * 32 + 2 * (lane & 15) + ((lane >> 4) & 1)) * 16;
      publish(ws, vp, WS_QP + cu * 2048, pack(a0, g)); publish(ws, vp, WS_QP + cu * 2048 + 1024, pack(a1, g));
      ST(1);
      v4u u[16]; const unsigned vg = (lane >> 5) * 1024 + b * 512 + (lane & 31) * 16;
      sweep<16>(ws, u, g, [&](int i, unsigned& v, unsigned& so) { v = vg; so = WS_QP + (kvh * 16 + i) * 2048; });
      f32x4 a = asf4(u[0]);
#pragma unroll
      for (int i = 1; i < 16; ++i) a += asf4(u[i]);
      *lp<v2u>(L_Q + lane * 8) = v2u{cvtpk(a[0], a[1]), cvtpk(a[2], a[3])};
      *lp<v4u>(L_TAG) = v4u{g.t[0], g.t[1], g.t[2], g.t[3]};
      lds_inc(L_QF);
      __builtin_amdgcn_s_setprio(0);
      ST(2);
    } else {
      asm volatile("s_barrier" ::: "memory");
    }
    if (isO) {
      v4u wv[16]; lds_wait_slow(L_CNTI, 8); ST(3);
      down_load<8>(wv, mkrs(wo, 4096 * D * 2), oi * 64 * (D * 2), w, lane);
      lds_wait_slow(L_QF, 1); g = lds_tag();
      asm volatile("s_waitcnt vmcnt(0)" ::: "memory"); lds_inc(L_CNTW); ST(6);
      lds_wait(L_OF, 1); ST(7);
      down_full<8, OH, OB>(wv, L_DI, ws, WS_OP + oi * 4608, w, lane, g); ST(8);
    } else if (isF) {
      const unsigned img = L_GI + w * 2 * GH;
      lds_zero(img, 2 * GH / 16, lane);
      const int gb = lane >> 4;
      const GLB char* wp = (const GLB char*)((lane & 4) ? w3 : w1) + (size_t)(144 * w + 36 * (gb >> 1) + 18 * ((lane >> 3) & 1) + (gb & 1)) * (FF * 2) + fi * 64 + (lane & 3) * 16;
      v4u wv[18]; lds_wait_slow(L_CNTI, 8); ST(3);
#pragma unroll
      for (int i = 0; i < 18; ++i) wv[i] = __builtin_nontemporal_load((const GLB v4u*)(wp + (72 * (i & 1) + 2 * (i >> 1)) * (FF * 2)));
      PIN();
      lds_wait_slow(L_QF, 1); g = lds_tag();
      const unsigned idx = (lane & 3) * 0x5555u, one = (lane & 4) ? 0u : 0x3f80u;
      { f32x4 cs = {0, 0, 0, 0}; const v4u A1 = {one, one ^ 0x3f80u, one, one ^ 0x3f80u};
#pragma unroll
        for (int u = 0; u < 9; ++u) cs = sm64(A1, wv[2 * u], wv[2 * u + 1], cs, idx);
        lds_inc(L_CNTW); ST(6);
        f32x4 r;
#pragma unroll
        for (int k = 0; k < 4; ++k) { auto sp = __builtin_amdgcn_permlane32_swap(asu(cs[k]), asu(cs[k]), false, false); r[k] = asf(sp[0]) + dpp<0x128>(asf(sp[1])); }
        if (!(lane & 40)) *lp<f32x4>(L_CS + w * 256 + ((lane >> 2) & 1) * 128 + (lane & 3) * 32 + ((lane >> 4) & 1) * 16) = r; }
      const int gi = lane < 36 ? lane : 35;
      v4u u[2]; sweep<2>(ws, u, g, [&](int i, unsigned& v, unsigned& so) { v = gi * 16; so = WS_HB + (w * 36 + i * 144) * 16; });
      ST(7);
#pragma unroll
      for (int bb = 0; bb < 2; ++bb)
#pragma unroll
        for (int k = 0; k < 4; ++k) { unsigned cc = hilo(asf(u[bb][k])), ad = img + bb * 1152 + gi * 32 + k * 8; *lp<unsigned>(ad) = cc; *lp<unsigned>(ad + GH + 4) = cc; }
      const unsigned ia = img + ((lane >> 2) & 1) * GH + (2 * (lane >> 4) + ((lane >> 3) & 1)) * 144;
      f32x4 a0 = {0, 0, 0, 0}, a1 = {0, 0, 0, 0};
#pragma unroll
      for (int t = 0; t < 9; ++t) { a0 = sm64(*lp<v4u>(ia + t * 16), wv[2 * t], wv[2 * t + 1], a0, idx); a1 = sm64(*lp<v4u>(ia + 1152 + t * 16), wv[2 * t], wv[2 * t + 1], a1, idx); }
      f32x4 r;
#pragma unroll
      for (int k = 0; k < 4; ++k) { auto sp = __builtin_amdgcn_permlane32_swap(asu(a0[k]), asu(a1[k]), false, false); r[k] = asf(sp[0]) + dpp<0x128>(asf(sp[1])); }
      if (!(lane & 8)) *lp<f32x4>(L_GP + w * 512 + (lane >> 5) * 256 + ((lane >> 2) & 1) * 128 + (lane & 3) * 32 + ((lane >> 4) & 1) * 16) = r;
      lds_inc(L_CNTG); ST(8);
    }
  }
}

extern "C" void kernel_launch(void* const* d_in, const int* in_sizes, int n_in, void* d_out, int out_size, void* d_ws, size_t ws_size, hipStream_t stream) {
  fused_decode_kernel<<<256, 768, 0, stream>>>((const u16*)d_in[0], (const u16*)d_in[1], (const u16*)d_in[2], (const u16*)d_in[3], (const u16*)d_in[4],
                                               (const u16*)d_in[6], (const u16*)d_in[7], (const u16*)d_in[8], (u16*)d_out, (unsigned*)d_ws);
}
```

```cpp
#include <hip/hip_runtime.h>
#include <hip/hip_bf16.h>
#include <stdint.h>

typedef unsigned v2u __attribute__((ext_vector_type(2)));
typedef unsigned v4u __attribute__((ext_vector_type(4)));
typedef unsigned v8u __attribute__((ext_vector_type(8)));
typedef float f32x2 __attribute__((ext_vector_type(2)));
typedef float f32x4 __attribute__((ext_vector_type(4)));
typedef short s16x4 __attribute__((ext_vector_type(4)));
typedef short s16x8 __attribute__((ext_vector_type(8)));
typedef __bf16 bf16x2_t __attribute__((ext_vector_type(2)));
typedef __bf16 bf16x8_t __attribute__((ext_vector_type(8)));
typedef __bf16 bf16x16_t __attribute__((ext_vector_type(16)));
typedef unsigned short u16;
typedef __amdgpu_buffer_rsrc_t rsrc_t;
#define LDS __attribute__((address_space(3)))
#define GLB __attribute__((address_space(1)))
#define DEV __device__ __forceinline__
#define PIN() __builtin_amdgcn_sched_barrier(0)

constexpr int D = 576, SK = 32768, WIN = 4096, FF = 4608;
constexpr int NLF = 17, NLO = 13;
constexpr int AUX_NT = 2, AUX_SC1 = 16;
#define ROT_C 5
#define ROT_W 15
constexpr unsigned WS_QP = 4096;
constexpr unsigned WS_AP = WS_QP + 256 * 2048;
constexpr unsigned WS_OP = WS_AP + 256 * 1152;
constexpr unsigned WS_HB = WS_OP + 64 * 4608;
constexpr unsigned WS_DP = WS_HB + 4608;
constexpr unsigned WS_END = WS_DP + 144 * 4608;
constexpr unsigned L_CNTI = 0, L_QF = 4, L_CNTM = 8, L_CNTF = 12, L_CNTW = 16, L_CNTG = 20, L_OF = 24;
constexpr unsigned L_TAG = 32;
constexpr unsigned L_ST = 48;
constexpr unsigned L_MX = 64;
constexpr unsigned L_L1 = 128;
constexpr unsigned L_Q = 192;
constexpr unsigned L_P1 = 704;
constexpr unsigned L_GP = L_P1 + 8192;
constexpr unsigned L_CS = L_GP + 2048;
constexpr unsigned GH = 2336;
constexpr unsigned L_GI = L_CS + 1024;
constexpr unsigned OH = 1024 + 64, OB = 512 + 32;
constexpr unsigned SH = 512 + 64, SB = 256 + 32;
constexpr unsigned L_DI = L_GI + 4 * 2 * GH;
constexpr unsigned SI = 2 * SH + 64;
constexpr unsigned L_END = L_DI + 5 * SI;
constexpr unsigned SPIN_G = 1u << 18, SPIN_L = 1u << 21;

#define ST_DECL
#define ST(i)
#define ST_PARAM
#define ST_ARG

DEV float asf(unsigned u) { return __builtin_bit_cast(float, u); }
DEV unsigned asu(float f) { return __builtin_bit_cast(unsigned, f); }
DEV float bflo(unsigned w) { return asf(w << 16); }
DEV float bfhi(unsigned w) { return asf(w & 0xffff0000u); }
DEV unsigned cvtpk(float lo, float hi) { return __builtin_bit_cast(unsigned, __builtin_convertvector(f32x2{lo, hi}, bf16x2_t)); }
DEV unsigned hilo(float v) { float h = asf(asu(v) & 0xffff0000u); return cvtpk(h, v - h); }
DEV f32x4 mfma32(v4u a, v4u b, f32x4 c) {
  return __builtin_amdgcn_mfma_f32_16x16x32_bf16(__builtin_bit_cast(bf16x8_t, a), __builtin_bit_cast(bf16x8_t, b), c, 0, 0, 0);
}
DEV f32x4 sm32(v2u a, v4u b, f32x4 c, unsigned idx) {
  return __builtin_amdgcn_smfmac_f32_16x16x32_bf16(__builtin_bit_cast(s16x4, a), __builtin_bit_cast(s16x8, b), c, idx, 0, 0);
}
DEV f32x4 sm64(v4u a, v4u b0, v4u b1, f32x4 c, unsigned idx) {
  v8u b = __builtin_shufflevector(b0, b1, 0, 1, 2, 3, 4, 5, 6, 7);
  return __builtin_amdgcn_smfmac_f32_16x16x64_bf16(__builtin_bit_cast(bf16x8_t, a), __builtin_bit_cast(bf16x16_t, b), c, idx, 0, 0);
}
template <int CTRL> DEV float dpp(float x) { return asf(__builtin_amdgcn_mov_dpp(asu(x), CTRL, 0xf, 0xf, true)); }
DEV float sw32add(float a, float b) { auto r = __builtin_amdgcn_permlane32_swap(asu(a), asu(b), false, false); return asf(r[0]) + asf(r[1]); }
DEV float sw16add(float a, float b) { auto r = __builtin_amdgcn_permlane16_swap(asu(a), asu(b), false, false); return asf(r[0]) + asf(r[1]); }
DEV float sw32max(float a) { auto r = __builtin_amdgcn_permlane32_swap(asu(a), asu(a), false, false); return fmaxf(asf(r[0]), asf(r[1])); }
DEV float sw16max(float a) { auto r = __builtin_amdgcn_permlane16_swap(asu(a), asu(a), false, false); return fmaxf(asf(r[0]), asf(r[1])); }
DEV float sum32(float v) {
  v += dpp<0xB1>(v); v += dpp<0x4E>(v); v += dpp<0x124>(v); v += dpp<0x128>(v); return sw16add(v, v);
}
DEV rsrc_t mkrs(const void* p, unsigned n) { return __builtin_amdgcn_make_buffer_rsrc((void*)p, 0, n, 0x00020000); }
template <int AUX> DEV v4u ld16(rsrc_t r, unsigned vo, unsigned so) { return __builtin_amdgcn_raw_buffer_load_b128(r, vo, so, AUX); }

struct Tag { unsigned t[4]; };
DEV unsigned tagw(float v, unsigned t) { return (asu(v) & 0xfffff800u) | (t | 0x400u); }
DEV v4u pack(f32x4 v, const Tag& g) { return v4u{tagw(v[0], g.t[0]), tagw(v[1], g.t[1]), tagw(v[2], g.t[2]), tagw(v[3], g.t[3])}; }
DEV void publish(rsrc_t ws, unsigned vo, unsigned so, v4u d) { __builtin_amdgcn_raw_buffer_store_b128(d, ws, vo, so, AUX_SC1); }
template <int N, typename FO> DEV void sweep(rsrc_t ws, v4u (&u)[N], const Tag& g, FO off) {
#pragma unroll 1
  for (unsigned spin = 0; spin < SPIN_G; ++spin) {
    unsigned bad = 0;
#pragma unroll
    for (int i = 0; i < N; ++i) { unsigned vo, so; off(i, vo, so); u[i] = ld16<AUX_SC1>(ws, vo, so); }
    PIN();
#pragma unroll
    for (int i = 0; i < N; ++i) { u[i] ^= v4u{g.t[0], g.t[1], g.t[2], g.t[3]}; bad |= u[i][0] | u[i][1] | u[i][2] | u[i][3]; }
    if (!__builtin_amdgcn_ballot_w64((bad & 0x3ffu) != 0)) break;
  }
}
DEV f32x4 asf4(v4u u) { return __builtin_bit_cast(f32x4, u); }

template <typename T> DEV LDS T* lp(unsigned off) { return (LDS T*)(uintptr_t)off; }
DEV void lds_inc(unsigned off) { asm volatile("s_mov_b64 exec, 1\n\tds_add_u32 %0, %1\n\ts_mov_b64 exec, -1" ::"v"(off), "v"(1u) : "memory"); }
DEV unsigned lds_inc_rtn(unsigned off) { unsigned o;
  asm volatile("s_mov_b64 exec, 1\n\tds_add_rtn_u32 %0, %1, %2\n\ts_mov_b64 exec, -1\n\ts_waitcnt lgkmcnt(0)" : "=v"(o) : "v"(off), "v"(1u) : "memory");
  return __builtin_amdgcn_readfirstlane(o); }
template <int SLEEP> DEV void lds_spin(unsigned off, unsigned val) { unsigned v, n;
  asm volatile("s_mov_b32 %1, %4\n"
               ".Lspin%=:\n\tds_read_b32 %0, %2\n\ts_sub_u32 %1, %1, 1\n\ts_waitcnt lgkmcnt(0)\n\ts_cbranch_scc1 .Ldone%=\n\t"
               "v_cmp_gt_u32 vcc, %3, %0\n\ts_cbranch_vccz .Ldone%=\n\ts_sleep %5\n\ts_branch .Lspin%=\n"
               ".Ldone%=:" : "=&v"(v), "=&s"(n) : "v"(off), "s"(val), "n"(SPIN_L), "n"(SLEEP) : "memory", "vcc", "scc"); }
DEV void lds_wait(unsigned off, unsigned val) { lds_spin<0>(off, val); }
DEV void lds_wait_slow(unsigned off, unsigned val) { lds_spin<2>(off, val); }
DEV Tag lds_tag() { v4u t = *lp<v4u>(L_TAG); Tag g;
#pragma unroll
  for (int i = 0; i < 4; ++i) g.t[i] = __builtin_amdgcn_readfirstlane(t[i]);
  return g; }

template <int NU> DEV void down_load(v4u (&w)[2 * NU], rsrc_t rw, unsigned base, int cb, int lane) {
  const int gb = lane >> 4;
  const unsigned vo = base + (2 * NU * (gb >> 1) + (gb & 1)) * (D * 2) + cb * 256 + (lane & 15) * 16;
#pragma unroll
  for (int i = 0; i < 2 * NU; ++i) w[i] = ld16<AUX_NT>(rw, vo, (4 * NU * (i & 1) + 2 * (i >> 1)) * (D * 2));
  PIN();
}
template <int NU, unsigned HS, unsigned BS> DEV void down_full(const v4u (&w)[2 * NU], unsigned img, rsrc_t ws, unsigned dst, int cb, int lane, const Tag& g) {
  const unsigned ia = img + ((lane >> 2) & 1) * HS + ((lane >> 3) & 1) * BS + (lane >> 4) * NU * 16, idx = (lane & 3) * 0x5555u;
  f32x4 acc = {0, 0, 0, 0};
#pragma unroll
  for (int u = 0; u < NU; ++u) acc = sm64(*lp<v4u>(ia + u * 16), w[2 * u], w[2 * u + 1], acc, idx);
  publish(ws, ((lane >> 5) * 144 + 2 * (lane & 15) + ((lane >> 4) & 1)) * 16, dst + cb * 512, pack(acc, g));
}
template <int NU> DEV void tail_load(v4u (&w)[NU], rsrc_t rw, unsigned base, int lane) {
  const int gb = lane >> 4;
  const unsigned vo = base + (NU * (2 * (gb >> 1) + ((lane >> 3) & 1)) + (gb & 1)) * (D * 2) + 1024 + (lane & 7) * 16;
#pragma unroll
  for (int i = 0; i < NU; ++i) w[i] = ld16<AUX_NT>(rw, vo, (4 * NU * (i & 1) + 2 * (i >> 1)) * (D * 2));
  PIN();
}
template <int NU, unsigned HS, unsigned BS> DEV void down_tail(const v4u (&w)[NU], unsigned img, rsrc_t ws, unsigned dst, int lane, const Tag& g) {
  const unsigned ia = img + ((lane >> 2) & 1) * HS + (2 * (lane >> 4) + ((lane >> 3) & 1)) * (NU / 2) * 16, idx = (lane & 3) * 0x5555u;
  f32x4 a0 = {0, 0, 0, 0}, a1 = {0, 0, 0, 0};
#pragma unroll
  for (int u = 0; u < NU / 2; ++u) { a0 = sm64(*lp<v4u>(ia + u * 16), w[2 * u], w[2 * u + 1], a0, idx); a1 = sm64(*lp<v4u>(ia + BS + u * 16), w[2 * u], w[2 * u + 1], a1, idx); }
  f32x4 r;
#pragma unroll
  for (int k = 0; k < 4; ++k) { auto s = __builtin_amdgcn_permlane32_swap(asu(a0[k]), asu(a1[k]), false, false); r[k] = asf(s[0]) + dpp<0x128>(asf(s[1])); }
  if (!(lane & 8)) publish(ws, ((lane >> 5) * 144 + 128 + 2 * (lane & 7) + ((lane >> 4) & 1)) * 16, dst, pack(r, g));
}
DEV void lds_zero(unsigned off, int n16, int lane) { for (int i = lane; i < n16; i += 64) *lp<v4u>(off + i * 16) = v4u{0, 0, 0, 0}; }

DEV void silu_stage(unsigned img, int lane) {
  lds_wait(L_CNTG, 5);
  const int b = lane >> 5, un = lane & 31;
  float g = 0, u = 0, cg = 0, cu = 0;
#pragma unroll
  for (int a = 0; a < 4; ++a) { g += *lp<float>(L_GP + a * 512 + b * 256 + un * 4); u += *lp<float>(L_GP + a * 512 + b * 256 + 128 + un * 4);
    cg += *lp<float>(L_CS + a * 256 + un * 4); cu += *lp<float>(L_CS + a * 256 + 128 + un * 4); }
  f32x2 st = *lp<f32x2>(L_ST + b * 8);
  g = (g - st[0] * cg) * st[1]; u = (u - st[0] * cu) * st[1];
  float s = g * u * __builtin_amdgcn_rcpf(1.f + __builtin_amdgcn_exp2f(g * -1.4426950408889634f));
  unsigned c = hilo(s), ad = img + b * SB + un * 8;
  *lp<unsigned>(ad) = c; *lp<unsigned>(ad + SH + 4) = c;
}

template <int NL> DEV void attention(const u16* kc, const u16* vc, rsrc_t ws, unsigned base, int rot, int sw, int cu, int lane, Tag& g ST_PARAM) {
  const rsrc_t rk = mkrs(kc, 2u * 16 * SK * 256), rv = mkrs(vc, 2u * 16 * SK * 256);
  const unsigned vk = base + ((lane >> 2) & 3) * 256 + (4 * (lane >> 4) + (lane & 3)) * 16, vv = base + lane * 16;
  v4u K[NL], V[NL]; unsigned so[NL];
  { unsigned s = rot * 1024;
#pragma unroll
    for (int t = 0; t < NL; ++t) { so[t] = s; s += 1024; if (s == NL * 1024) s = 0; } }
#pragma unroll
  for (int t = 0; t < 4; ++t) K[t] = ld16<AUX_NT>(rk, vk, so[t]);
  PIN(); asm volatile("s_barrier" ::: "memory"); PIN();
#pragma unroll
  for (int t = 4; t < NL; ++t) K[t] = ld16<AUX_NT>(rk, vk, so[t]);
  PIN();
#pragma unroll
  for (int t = 0; t < NL; ++t) V[t] = ld16<AUX_NT>(rv, vv, so[t]);
  PIN();
  lds_inc(L_CNTI);
  ST(1);
  lds_wait_slow(L_QF, 1);
  ST(2);
  g = lds_tag();
  const v4u qb = *lp<v4u>(L_Q + ((lane >> 3) & 1) * 256 + (4 * (lane >> 4) + (lane & 3)) * 16);
  constexpr float C2 = 0.08838834764831845f * 1.4426950408889634f;
  float sc[NL], mx = -INFINITY;
#pragma unroll
  for (int t = 0; t < NL; ++t) {
    f32x4 d = mfma32(K[t], qb, f32x4{0, 0, 0, 0});
    sc[t] = (dpp<0x00>(d[0]) + dpp<0x55>(d[1]) + dpp<0xAA>(d[2]) + dpp<0xFF>(d[3])) * C2;
    mx = fmaxf(mx, sc[t]);
  }
  mx = sw32max(sw16max(mx));
  const int hs = (lane >> 3) & 1, ha = lane >> 5;
  *lp<float>(L_MX + sw * 8 + hs * 4) = mx;
  f32x4 acc = {0, 0, 0, 0}; float l = 0;
  ST(3);
  if (sw >= 4) {
    lds_inc(L_CNTM);
    lds_wait(L_CNTM, 8);
    float ma = -INFINITY;
#pragma unroll
    for (int i = 0; i < 8; ++i) { mx = fmaxf(mx, *lp<float>(L_MX + i * 8 + hs * 4)); ma = fmaxf(ma, *lp<float>(L_MX + i * 8 + ha * 4)); }
    acc = *lp<f32x4>(L_P1 + (sw - 4) * 1024 + lane * 16) * __builtin_amdgcn_exp2f(*lp<float>(L_MX + (sw - 4) * 8 + ha * 4) - ma);
    if (lane < 16) l = *lp<float>(L_L1 + (sw - 4) * 8 + hs * 4) * __builtin_amdgcn_exp2f(*lp<float>(L_MX + (sw - 4) * 8 + hs * 4) - mx);
  }
  const unsigned mA = (lane & 4) ? 0u : ~0u, idx = (lane & 3) * 0x5555u;
  v2u A[NL];
#pragma unroll
  for (int t = 0; t < NL; ++t) { float p = __builtin_amdgcn_exp2f(sc[t] - mx); l += p; unsigned c = hilo(p); A[t] = v2u{c & mA, c & ~mA}; }
  l = sw16add(l, l); l = sw32add(l, l);
  *lp<float>(L_L1 + sw * 8 + hs * 4) = l;
  __builtin_amdgcn_sched_barrier(0);
#pragma unroll
  for (int t = 0; t < NL; ++t) acc = sm32(A[t], V[t], acc, idx);
  *lp<f32x4>(L_P1 + sw * 1024 + lane * 16) = acc;
  ST(4);
  if (sw < 4) { lds_inc(L_CNTM); return; }
  if (lds_inc_rtn(L_CNTF) == 3) {
    __builtin_amdgcn_s_setprio(3);
    f32x4 o = *lp<f32x4>(L_P1 + 4096 + lane * 16); float L = *lp<float>(L_L1 + 32 + hs * 4);
#pragma unroll
    for (int i = 1; i < 4; ++i) { o += *lp<f32x4>(L_P1 + 4096 + i * 1024 + lane * 16); L += *lp<float>(L_L1 + 32 + i * 8 + hs * 4); }
    publish(ws, (ha * 32 + 2 * (lane & 15) + ((lane >> 4) & 1)) * 16, WS_AP + cu * 1152, pack(o, g));
    v4u ml = pack(f32x4{mx, asf(asu(mx) << 21), L, L}, g);
    if (lane < 16 && !(lane & 4)) publish(ws, 1024 + (hs * 4 + (lane & 3)) * 16, WS_AP + cu * 1152, ml);
    __builtin_amdgcn_s_setprio(0);
  }
  ST(5);
}

__global__ void __launch_bounds__(768, 3)
fused_decode_kernel(const u16* __restrict__ x, const u16* __restrict__ kc, const u16* __restrict__ vc, const u16* __restrict__ wq, const u16* __restrict__ wo,
                    const u16* __restrict__ w1, const u16* __restrict__ w3, const u16* __restrict__ w2, u16* __restrict__ out, unsigned* __restrict__ wsp) {
  __shared__ v4u lds_all[L_END / 16 + 1];
  unsigned long long tstart_; asm volatile("s_memrealtime %0\n\ts_waitcnt lgkmcnt(0)" : "=s"(tstart_) :: "memory"); PIN();
  asm volatile("" ::"s"((LDS v4u*)lds_all), "s"(x), "s"(kc), "s"(vc), "s"(wq), "s"(wo), "s"(w1), "s"(w3), "s"(w2), "s"(out), "s"(wsp));
  ST_DECL; ST(0);
  const int lane = threadIdx.x & 63, w = __builtin_amdgcn_readfirstlane(threadIdx.x >> 6), c = blockIdx.x;
  const int kvh = 2 * (c & 7) + (c >> 7), j = (c >> 3) & 15, b = j >> 3, s = j & 7, cu = kvh * 16 + j;
  const bool isO = s < 2, isF = !isO && s < 7 - b;
  const int oi = kvh * 4 + b * 2 + s, fi = kvh * 9 + b * 5 + s - 2, ri = kvh * 3 + (b ? s - 5 : 0);
  const rsrc_t ws = mkrs(wsp, WS_END);
  Tag g;
  if (w >= 4) {
    const int sw = w - 4;
    const unsigned row0 = (unsigned)(b * 16 + kvh) * SK + (SK - WIN);
    if (isO) attention<NLO>(kc, vc, ws, (row0 + s * (32 * NLO) + sw * (4 * NLO)) * 256, (c * ROT_C + sw * ROT_W) % NLO, sw, cu, lane, g ST_ARG);
    else attention<NLF>(kc, vc, ws, (row0 + 64 * NLO + (s - 2) * (32 * NLF) + sw * (4 * NLF)) * 256, (c * ROT_C + sw * ROT_W) % NLF, sw, cu, lane, g ST_ARG);
    if (isO) {
      if (sw == 0) {
        v4u wt[8]; lds_wait_slow(L_CNTI, 8); tail_load<8>(wt, mkrs(wo, 4096 * D * 2), oi * 64 * (D * 2), lane);
        lds_wait(L_OF, 1); ST(7);
        down_tail<8, OH, OB>(wt, L_DI, ws, WS_OP + oi * 4608, lane, g); ST(8);
      } else if (sw == 4) {
        lds_zero(L_DI, (2 * OH) / 16, lane);
        lds_wait_slow(L_CNTW, 4); ST(6);
        const int jo = oi & 3; v4u u[8];
        const unsigned vo = (lane >> 4) * 1152 + (jo * 16 + (lane & 15)) * 16, vm = (lane >> 4) * 1152 + 1024 + (jo >> 1) * 64;
        sweep<8>(ws, u, g, [&](int i, unsigned& v, unsigned& so) { v = i < 4 ? vo : vm; so = WS_AP + (kvh * 16 + (i & 3) * 4) * 1152; });
        ST(7);
        f32x4 ob[2]; float Lb[2];
#pragma unroll
        for (int bb = 0; bb < 2; ++bb) {
          float m0 = asf((u[4 + 2 * bb][0] & 0xfffff800u) | (u[4 + 2 * bb][1] >> 21)), m1 = asf((u[5 + 2 * bb][0] & 0xfffff800u) | (u[5 + 2 * bb][1] >> 21));
          float M = sw32max(sw16max(fmaxf(m0, m1))), e0 = __builtin_amdgcn_exp2f(m0 - M), e1 = __builtin_amdgcn_exp2f(m1 - M);
          ob[bb] = asf4(u[2 * bb]) * e0 + asf4(u[2 * bb + 1]) * e1; Lb[bb] = asf(u[4 + 2 * bb][2]) * e0 + asf(u[5 + 2 * bb][2]) * e1;
        }
        float L = sw32add(Lb[0], Lb[1]); L = sw16add(L, L);
        float r0 = sw32add(ob[0][0], ob[1][0]), r1 = sw32add(ob[0][1], ob[1][1]), r2 = sw32add(ob[0][2], ob[1][2]), r3 = sw32add(ob[0][3], ob[1][3]);
        float inv = __builtin_amdgcn_rcpf(L);
        unsigned c0 = hilo(sw16add(r0, r1) * inv), c1 = hilo(sw16add(r2, r3) * inv);
        unsigned ad = L_DI + (lane >> 5) * OB + (lane & 15) * 32 + ((lane >> 4) & 1) * 8;
        *lp<unsigned>(ad) = c0; *lp<unsigned>(ad + 16) = c1; *lp<unsigned>(ad + OH + 4) = c0; *lp<unsigned>(ad + OH + 20) = c1;
        lds_inc(L_OF); ST(8);
      }
    } else if (isF) {
      if (sw < 4) {
        v4u wd[8]; lds_zero(L_DI + sw * SI, SI / 16, lane);
        lds_wait_slow(L_CNTI, 8); down_load<4>(wd, mkrs(w2, FF * D * 2), fi * 32 * (D * 2), sw, lane);
        ST(6);
        silu_stage(L_DI + sw * SI, lane); ST(7);
        down_full<4, SH, SB>(wd, L_DI + sw * SI, ws, WS_DP + fi * 4608, sw, lane, g); ST(8);
      } else if (sw == 5) {
        v4u wt[4]; tail_load<4>(wt, mkrs(w2, FF * D * 2), fi * 32 * (D * 2), lane); lds_zero(L_DI + 4 * SI, SI / 16, lane);
        silu_stage(L_DI + 4 * SI, lane); ST(7);
        down_tail<4, SH, SB>(wt, L_DI + 4 * SI, ws, WS_DP + fi * 4608, lane, g); ST(8);
      } else if (sw == 4) {
        lds_wait_slow(L_CNTW, 4); ST(6);
        v4u u[5]; const unsigned vo = ((lane >> 5) * 144 + (lane & 31)) * 16, vt = ((lane >> 5) * 144 + (lane & 15)) * 16;
        sweep<5>(ws, u, g, [&](int i, unsigned& v, unsigned& so) { v = i < 4 ? vo : vt; so = WS_HB + i * 512; });
        ST(7);
        const float wl = (lane & 16) ? 0.f : 1.f;
        f32x4 sv = asf4(u[0]) + asf4(u[1]) + asf4(u[2]) + asf4(u[3]) + asf4(u[4]) * wl;
        const float m = sum32(sv[0] + sv[1] + sv[2] + sv[3]) * (1.f / D);
        f32x4 q = {0, 0, 0, 0};
#pragma unroll
        for (int i = 0; i < 4; ++i) { f32x4 d = asf4(u[i]) - m; q += d * d; }
        { f32x4 d = asf4(u[4]) - m; q += d * d * wl; }
        const float var = sum32(q[0] + q[1] + q[2] + q[3]) * (1.f / D);
        *lp<f32x2>(L_ST + (lane >> 5) * 8) = f32x2{m, __builtin_amdgcn_rsqf(var + 1e-5f)};
        lds_inc(L_CNTG); ST(8);
      }
    } else if (sw == 4 && ri < 36) {
      const int rg = (lane >> 5) + 2 * ((lane >> 4) & 1), col = ri * 32 + (lane & 7) * 4 + rg;
      const unsigned tg = rg == 0 ? g.t[0] : rg == 1 ? g.t[1] : rg == 2 ? g.t[2] : g.t[3];
      const float xv = bflo(x[col]);
      const unsigned vo = (lane >> 3) * 4608 + (ri * 8 + (lane & 7)) * 16;
      float h;
      { v4u u[8]; sweep<8>(ws, u, g, [&](int i, unsigned& v, unsigned& so) { v = vo; so = WS_OP + i * 8 * 4608; });
        ST(6);
        f32x4 a = asf4(u[0]);
#pragma unroll
        for (int i = 1; i < 8; ++i) a += asf4(u[i]);
        h = sw16add(sw32add(a[0], a[1]), sw32add(a[2], a[3])); h += dpp<0x128>(h); h += xv;
        if (!(lane & 8)) __builtin_amdgcn_raw_buffer_store_b32(tagw(h, tg), ws, col * 4, WS_HB, AUX_SC1);
        ST(7); }
      { v4u u[18]; sweep<18>(ws, u, g, [&](int i, unsigned& v, unsigned& so) { v = vo; so = WS_DP + i * 8 * 4608; });
        ST(8);
        f32x4 a = asf4(u[0]);
#pragma unroll
        for (int i = 1; i < 18; ++i) a += asf4(u[i]);
        float y = sw16add(sw32add(a[0], a[1]), sw32add(a[2], a[3])); y += dpp<0x128>(y); y += h;
        if (!(lane & 8)) out[col] = (u16)cvtpk(y, y);
        if (ri == 0 && lane == 0) publish(ws, 0, 0, v4u{g.t[0], g.t[1], g.t[2], g.t[3]});
      }
    }
  } else {
    if (w == 1) { if (lane < 8) *lp<unsigned>(lane * 4) = 0; asm volatile("s_waitcnt lgkmcnt(0)" ::: "memory"); }
    if (w == 0) {
      __builtin_amdgcn_s_setprio(3);
      const rsrc_t rx = mkrs(x, 2 * D * 2), rq = mkrs(wq, D * 4096 * 2);
      v4u ep = ld16<AUX_SC1>(ws, 0, 0);
      unsigned xs[9], xa[9]; v4u wv[18];
#pragma unroll
      for (int k = 0; k < 9; ++k) xs[k] = __builtin_amdgcn_raw_buffer_load_b32(rx, (lane >> 5) * (D * 2) + (lane & 31) * 4, k * 128, 0);
#pragma unroll
      for (int t = 0; t < 9; ++t) xa[t] = __builtin_amdgcn_raw_buffer_load_b16(rx, (((lane >> 3) & 1) * D + 36 * j + (lane >> 4)) * 2, t * 8, 0);
      PIN();
      const unsigned vq = ((36 * j + (lane >> 4)) * 4096 + kvh * 256 + (lane & 15) * 8) * 2;
#pragma unroll
      for (int i = 0; i < 18; ++i) wv[i] = ld16<AUX_NT>(rq, vq, (i >> 1) * 4 * 8192 + (i & 1) * 256);
      PIN(); asm volatile("s_barrier" ::: "memory"); PIN();
#pragma unroll
      for (int i = 0; i < 4; ++i) g.t[i] = (__builtin_amdgcn_readfirstlane(ep[i]) + 1) & 0x3ffu;
      float sm = 0;
#pragma unroll
      for (int k = 0; k < 9; ++k) sm += bflo(xs[k]) + bfhi(xs[k]);
      const float m = sum32(sm) * (1.f / D); float q = 0;
#pragma unroll
      for (int k = 0; k < 9; ++k) { float d0 = bflo(xs[k]) - m, d1 = bfhi(xs[k]) - m; q += d0 * d0 + d1 * d1; }
      const float r = __builtin_amdgcn_rsqf(sum32(q) * (1.f / D) + 1e-5f);
      const float m0 = asf(__builtin_amdgcn_readlane(asu(m), 0)), m1 = asf(__builtin_amdgcn_readlane(asu(m), 32));
      const float r0 = asf(__builtin_amdgcn_readlane(asu(r), 0)), r1 = asf(__builtin_amdgcn_readlane(asu(r), 32));
      const float mm = (lane & 8) ? m1 : m0, rr = (lane & 8) ? r1 : r0;
      const unsigned mA = (lane & 4) ? 0u : ~0u, idx = (lane & 3) * 0x5555u;
      f32x4 a0 = {0, 0, 0, 0}, a1 = {0, 0, 0, 0};
#pragma unroll
      for (int t = 0; t < 9; ++t) { unsigned cc = hilo((bflo(xa[t]) - mm) * rr); v2u A = {cc & mA, cc & ~mA};
        a0 = sm32(A, wv[2 * t], a0, idx); a1 = sm32(A, wv[2 * t + 1], a1, idx); }
      const unsigned vp = ((lane >> 5) * 32 + 2 * (lane & 15) + ((lane >> 4) & 1)) * 16;
      publish(ws, vp, WS_QP + cu * 2048, pack(a0, g)); publish(ws, vp, WS_QP + cu * 2048 + 1024, pack(a1, g));
      ST(1);
      v4u u[16]; const unsigned vg = (lane >> 5) * 1024 + b * 512 + (lane & 31) * 16;
      sweep<16>(ws, u, g, [&](int i, unsigned& v, unsigned& so) { v = vg; so = WS_QP + (kvh * 16 + i) * 2048; });
      f32x4 a = asf4(u[0]);
#pragma unroll
      for (int i = 1; i < 16; ++i) a += asf4(u[i]);
      *lp<v2u>(L_Q + lane * 8) = v2u{cvtpk(a[0], a[1]), cvtpk(a[2], a[3])};
      *lp<v4u>(L_TAG) = v4u{g.t[0], g.t[1], g.t[2], g.t[3]};
      lds_inc(L_QF);
      __builtin_amdgcn_s_setprio(0);
      ST(2);
    } else {
      asm volatile("s_barrier" ::: "memory");
    }
    if (isO) {
      v4u wv[16]; lds_wait_slow(L_CNTI, 8); ST(3);
      down_load<8>(wv, mkrs(wo, 4096 * D * 2), oi * 64 * (D * 2), w, lane);
      lds_wait_slow(L_QF, 1); g = lds_tag();
      asm volatile("s_waitcnt vmcnt(0)" ::: "memory"); lds_inc(L_CNTW); ST(6);
      lds_wait(L_OF, 1); ST(7);
      down_full<8, OH, OB>(wv, L_DI, ws, WS_OP + oi * 4608, w, lane, g); ST(8);
    } else if (isF) {
      const unsigned img = L_GI + w * 2 * GH;
      lds_zero(img, 2 * GH / 16, lane);
      const int gb = lane >> 4;
      const GLB char* wp = (const GLB char*)((lane & 4) ? w3 : w1) + (size_t)(144 * w + 36 * (gb >> 1) + 18 * ((lane >> 3) & 1) + (gb & 1)) * (FF * 2) + fi * 64 + (lane & 3) * 16;
      v4u wv[18]; lds_wait_slow(L_CNTI, 8); ST(3);
#pragma unroll
      for (int i = 0; i < 18; ++i) wv[i] = __builtin_nontemporal_load((const GLB v4u*)(wp + (72 * (i & 1) + 2 * (i >> 1)) * (FF * 2)));
      PIN();
      lds_wait_slow(L_QF, 1); g = lds_tag();
      const unsigned idx = (lane & 3) * 0x5555u, one = (lane & 4) ? 0u : 0x3f80u;
      { f32x4 cs = {0, 0, 0, 0}; const v4u A1 = {one, one ^ 0x3f80u, one, one ^ 0x3f80u};
#pragma unroll
        for (int u = 0; u < 9; ++u) cs = sm64(A1, wv[2 * u], wv[2 * u + 1], cs, idx);
        lds_inc(L_CNTW); ST(6);
        f32x4 r;
#pragma unroll
        for (int k = 0; k < 4; ++k) { auto sp = __builtin_amdgcn_permlane32_swap(asu(cs[k]), asu(cs[k]), false, false); r[k] = asf(sp[0]) + dpp<0x128>(asf(sp[1])); }
        if (!(lane & 40)) *lp<f32x4>(L_CS + w * 256 + ((lane >> 2) & 1) * 128 + (lane & 3) * 32 + ((lane >> 4) & 1) * 16) = r; }
      const int gi = lane < 36 ? lane : 35;
      v4u u[2]; sweep<2>(ws, u, g, [&](int i, unsigned& v, unsigned& so) { v = gi * 16; so = WS_HB + (w * 36 + i * 144) * 16; });
      ST(7);
#pragma unroll
      for (int bb = 0; bb < 2; ++bb)
#pragma unroll
        for (int k = 0; k < 4; ++k) { unsigned cc = hilo(asf(u[bb][k])), ad = img + bb * 1152 + gi * 32 + k * 8; *lp<unsigned>(ad) = cc; *lp<unsigned>(ad + GH + 4) = cc; }
      const unsigned ia = img + ((lane >> 2) & 1) * GH + (2 * (lane >> 4) + ((lane >> 3) & 1)) * 144;
      f32x4 a0 = {0, 0, 0, 0}, a1 = {0, 0, 0, 0};
#pragma unroll
      for (int t = 0; t < 9; ++t) { a0 = sm64(*lp<v4u>(ia + t * 16), wv[2 * t], wv[2 * t + 1], a0, idx); a1 = sm64(*lp<v4u>(ia + 1152 + t * 16), wv[2 * t], wv[2 * t + 1], a1, idx); }
      f32x4 r;
#pragma unroll
      for (int k = 0; k < 4; ++k) { auto sp = __builtin_amdgcn_permlane32_swap(asu(a0[k]), asu(a1[k]), false, false); r[k] = asf(sp[0]) + dpp<0x128>(asf(sp[1])); }
      if (!(lane & 8)) *lp<f32x4>(L_GP + w * 512 + (lane >> 5) * 256 + ((lane >> 2) & 1) * 128 + (lane & 3) * 32 + ((lane >> 4) & 1) * 16) = r;
      lds_inc(L_CNTG); ST(8);
    }
  }
  if (c == 0 && w == 1 && lane == 0) *(unsigned long long*)(wsp + (2u << 20)) = tstart_;
}

__global__ void __launch_bounds__(64) k_tiny(unsigned* wsp) { unsigned long long t0 = *(unsigned long long*)(wsp + (2u << 20)); while (__builtin_amdgcn_s_memrealtime() - t0 < 4000) __builtin_amdgcn_s_sleep(1); }
extern "C" void kernel_launch(void* const* d_in, const int* in_sizes, int n_in, void* d_out, int out_size, void* d_ws, size_t ws_size, hipStream_t stream) {
  fused_decode_kernel<<<256, 768, 0, stream>>>((const u16*)d_in[0], (const u16*)d_in[1], (const u16*)d_in[2], (const u16*)d_in[3], (const u16*)d_in[4],
                                               (const u16*)d_in[6], (const u16*)d_in[7], (const u16*)d_in[8], (u16*)d_out, (unsigned*)d_ws);
  k_tiny<<<1, 64, 0, stream>>>((unsigned*)d_ws);
}
```

```cpp
#include <hip/hip_runtime.h>
#include <hip/hip_bf16.h>
#include <stdint.h>

typedef unsigned v2u __attribute__((ext_vector_type(2)));
typedef unsigned v4u __attribute__((ext_vector_type(4)));
typedef unsigned v8u __attribute__((ext_vector_type(8)));
typedef float f32x2 __attribute__((ext_vector_type(2)));
typedef float f32x4 __attribute__((ext_vector_type(4)));
typedef short s16x4 __attribute__((ext_vector_type(4)));
typedef short s16x8 __attribute__((ext_vector_type(8)));
typedef __bf16 bf16x2_t __attribute__((ext_vector_type(2)));
typedef __bf16 bf16x8_t __attribute__((ext_vector_type(8)));
typedef __bf16 bf16x16_t __attribute__((ext_vector_type(16)));
typedef unsigned short u16;
typedef __amdgpu_buffer_rsrc_t rsrc_t;
#define LDS __attribute__((address_space(3)))
#define GLB __attribute__((address_space(1)))
#define DEV __device__ __forceinline__
#define PIN() __builtin_amdgcn_sched_barrier(0)

constexpr int D = 576, SK = 32768, WIN = 4096, FF = 4608, NL = 16;
constexpr int AUX_NT = 2, AUX_SC1 = 16;
#define ROT_C 5
#define ROT_W 15
#define CAP 3
#define CAPO 4
#define QA 6
#define CAPW 4
#define TW 0
#define WOE 0
#define VUN 16
#define GATEW 0
#define W2G 0
#define TF0 5000
#define TFD 80
#define TO0 250
#define TOD 45
#define GATE C_I
#define GATEN 8
#define LAGO 16
#define MASKED 1
#define ODELAY 0
#define OSL 0
#define CAP2 8
#define NLOW 0
#define NWO 16
#define HG 1
#define RDELAY 0
constexpr unsigned WS_QP = 4096;
constexpr unsigned WS_AP = WS_QP + 16 * 8192;
constexpr unsigned WS_OP = WS_AP + 256 * 1152;
constexpr unsigned WS_GU = WS_OP + 8 * 4608;
constexpr unsigned WS_DP = WS_GU + 144 * 1024;
constexpr unsigned WS_END = WS_DP + 72 * 4608;
constexpr unsigned C_Q = 0, C_QF = 4, C_I = 8, C_M = 12, C_F = 16, C_Y = 20, C_W = 24, C_H = 28, C_G = 32, C_HB = 36, C_R = 40, C_WI = 44, C_WO = 24;
constexpr unsigned L_TAG = 48;
constexpr unsigned L_ST = 64;
constexpr unsigned L_MX = 128;
constexpr unsigned L_L1 = 192;
constexpr unsigned L_Q = 256;
constexpr unsigned L_QP = 768;
constexpr unsigned L_P1 = L_QP + 2048;
constexpr unsigned L_YP = L_P1 + 8192;
constexpr unsigned BST = 2688;
constexpr unsigned L_HI = L_YP + 4096;
constexpr unsigned L_GP = L_HI + 2 * BST;
constexpr unsigned L_CS = L_GP + 2048;
constexpr unsigned L_RP = L_CS + 1024;
constexpr unsigned L_HB = L_RP + 1152;
constexpr unsigned L_WO = L_HB + 128;
constexpr unsigned L_END = L_WO + 65536;
constexpr unsigned SPIN_G = 1u << 18, SPIN_L = 1u << 21;

#define ST_DECL
#define ST(i)
#define ST_PARAM
#define ST_ARG

DEV float asf(unsigned u) { return __builtin_bit_cast(float, u); }
DEV unsigned asu(float f) { return __builtin_bit_cast(unsigned, f); }
DEV float bflo(unsigned w) { return asf(w << 16); }
DEV float bfhi(unsigned w) { return asf(w & 0xffff0000u); }
DEV unsigned cvtpk(float lo, float hi) { return __builtin_bit_cast(unsigned, __builtin_convertvector(f32x2{lo, hi}, bf16x2_t)); }
DEV unsigned hilo(float v) { float h = asf(asu(v) & 0xffff0000u); return cvtpk(h, v - h); }
DEV f32x4 mfma32(v4u a, v4u b, f32x4 c) {
  return __builtin_amdgcn_mfma_f32_16x16x32_bf16(__builtin_bit_cast(bf16x8_t, a), __builtin_bit_cast(bf16x8_t, b), c, 0, 0, 0);
}
DEV f32x4 sm32(v2u a, v4u b, f32x4 c, unsigned idx) {
  return __builtin_amdgcn_smfmac_f32_16x16x32_bf16(__builtin_bit_cast(s16x4, a), __builtin_bit_cast(s16x8, b), c, idx, 0, 0);
}
DEV f32x4 sm64(v4u a, v8u b, f32x4 c, unsigned idx) {
  return __builtin_amdgcn_smfmac_f32_16x16x64_bf16(__builtin_bit_cast(bf16x8_t, a), __builtin_bit_cast(bf16x16_t, b), c, idx, 0, 0);
}
template <int CTRL> DEV unsigned dppu(unsigned x) { return __builtin_amdgcn_mov_dpp(x, CTRL, 0xf, 0xf, true); }
template <int CTRL> DEV float dpp(float x) { return asf(dppu<CTRL>(asu(x))); }
DEV v4u quad(unsigned c) { return v4u{dppu<0x00>(c), dppu<0x55>(c), dppu<0xAA>(c), dppu<0xFF>(c)}; }
DEV float sw32add(float a, float b) { auto r = __builtin_amdgcn_permlane32_swap(asu(a), asu(b), false, false); return asf(r[0]) + asf(r[1]); }
DEV float sw16add(float a, float b) { auto r = __builtin_amdgcn_permlane16_swap(asu(a), asu(b), false, false); return asf(r[0]) + asf(r[1]); }
DEV float sw32max(float a) { auto r = __builtin_amdgcn_permlane32_swap(asu(a), asu(a), false, false); return fmaxf(asf(r[0]), asf(r[1])); }
DEV float sw16max(float a) { auto r = __builtin_amdgcn_permlane16_swap(asu(a), asu(a), false, false); return fmaxf(asf(r[0]), asf(r[1])); }
DEV float sum32(float v) {
  v += dpp<0xB1>(v); v += dpp<0x4E>(v); v += dpp<0x124>(v); v += dpp<0x128>(v); return sw16add(v, v);
}
DEV rsrc_t mkrs(const void* p, unsigned n) { return __builtin_amdgcn_make_buffer_rsrc((void*)p, 0, n, 0x00020000); }
template <int AUX> DEV v4u ld16(rsrc_t r, unsigned vo, unsigned so) { return __builtin_amdgcn_raw_buffer_load_b128(r, vo, so, AUX); }
template <int N> DEV void vmw() { PIN(); __builtin_amdgcn_s_waitcnt((N & 15) | 0x0f70 | ((N >> 4) << 14)); PIN(); }
DEV void gap() { PIN(); asm volatile("s_nop 7"); PIN(); }
DEV f32x4 asf4(v4u u) { return __builtin_bit_cast(f32x4, u); }

struct Tag { unsigned t[4]; };
DEV unsigned tagw(float v, unsigned t) { return (asu(v) & 0xfffff800u) | t; }
DEV v4u pack(f32x4 v, const Tag& g) { return v4u{tagw(v[0], g.t[0]), tagw(v[1], g.t[1]), tagw(v[2], g.t[2]), tagw(v[3], g.t[3])}; }
DEV void publish(rsrc_t ws, unsigned vo, unsigned so, v4u d) { __builtin_amdgcn_raw_buffer_store_b128(d, ws, vo, so, AUX_SC1); }
template <int N, int SLEEP, typename FO> DEV void sweep(rsrc_t ws, v4u (&u)[N], const Tag& g, FO off) {
  bool bad[N];
#pragma unroll
  for (int i = 0; i < N; ++i) bad[i] = true;
#pragma unroll 1
  for (unsigned spin = 0; spin < SPIN_G; ++spin) {
#pragma unroll
    for (int i = 0; i < N; ++i) { unsigned vo, so; off(i, vo, so); if (bad[i]) u[i] = ld16<AUX_SC1>(ws, vo, so); }
    PIN();
    bool any = false;
#pragma unroll
    for (int i = 0; i < N; ++i) { bad[i] = (((u[i][0] ^ g.t[0]) | (u[i][1] ^ g.t[1]) | (u[i][2] ^ g.t[2]) | (u[i][3] ^ g.t[3])) & 0x7ffu) != 0; any |= bad[i]; }
    if (!__builtin_amdgcn_ballot_w64(any)) break;
    if (SLEEP) __builtin_amdgcn_s_sleep(SLEEP);
  }
  return;
#pragma unroll 1
  for (unsigned spin = 0; spin < SPIN_G; ++spin) {
    unsigned bad = 0;
#pragma unroll
    for (int i = 0; i < N; ++i) { unsigned vo, so; off(i, vo, so); u[i] = ld16<AUX_SC1>(ws, vo, so); }
    PIN();
#pragma unroll
    for (int i = 0; i < N; ++i)
#pragma unroll
      for (int k = 0; k < 4; ++k) bad |= u[i][k] ^ g.t[k];
    if (!__builtin_amdgcn_ballot_w64((bad & 0x7ffu) != 0)) break;
    if (SLEEP) __builtin_amdgcn_s_sleep(SLEEP);
  }
}

template <typename T> DEV LDS T* lp(unsigned off) { return (LDS T*)(uintptr_t)off; }
DEV void lds_inc(unsigned off) { asm volatile("s_mov_b64 exec, 1\n\tds_add_u32 %0, %1\n\ts_mov_b64 exec, -1" ::"v"(off), "v"(1u) : "memory"); }
DEV unsigned lds_inc_rtn(unsigned off) { unsigned o;
  asm volatile("s_mov_b64 exec, 1\n\tds_add_rtn_u32 %0, %1, %2\n\ts_mov_b64 exec, -1\n\ts_waitcnt lgkmcnt(0)" : "=v"(o) : "v"(off), "v"(1u) : "memory");
  return __builtin_amdgcn_readfirstlane(o); }
template <int SLEEP> DEV void lds_spin(unsigned off, unsigned val) { unsigned v, n;
  asm volatile("s_mov_b32 %1, %4\n"
               ".Lspin%=:\n\tds_read_b32 %0, %2\n\ts_sub_u32 %1, %1, 1\n\ts_waitcnt lgkmcnt(0)\n\ts_cbranch_scc1 .Ldone%=\n\t"
               "v_cmp_gt_u32 vcc, %3, %0\n\ts_cbranch_vccz .Ldone%=\n\ts_sleep %5\n\ts_branch .Lspin%=\n"
               ".Ldone%=:" : "=&v"(v), "=&s"(n) : "v"(off), "s"(val), "n"(SPIN_L), "n"(SLEEP) : "memory", "vcc", "scc"); }
DEV void lds_wait(unsigned off, unsigned val) { lds_spin<0>(off, val); }
DEV void lds_wait_slow(unsigned off, unsigned val) { lds_spin<2>(off, val); }
DEV void pace(unsigned t0, unsigned ticks, bool& done) {
  while (!done && (unsigned)__builtin_amdgcn_s_memrealtime() - t0 < ticks) {
    done = __builtin_amdgcn_readfirstlane(*(volatile LDS unsigned*)(uintptr_t)GATE) >= GATEN;
    if (!done) __builtin_amdgcn_s_sleep(1);
  }
  PIN();
}
DEV Tag lds_tag() { v4u t = *lp<v4u>(L_TAG); Tag g;
#pragma unroll
  for (int i = 0; i < 4; ++i) g.t[i] = __builtin_amdgcn_readfirstlane(t[i]);
  return g; }

struct Quad { v8u x, y; };
DEV Quad regroup(v4u a, v4u b, v4u c, v4u d) {
  Quad q; q.x = v8u{a[0], a[1], b[0], b[1], c[0], c[1], d[0], d[1]}; q.y = v8u{a[2], a[3], b[2], b[3], c[2], c[3], d[2], d[3]};
  asm volatile("" : "+v"(q.x), "+v"(q.y)); return q;
}
DEV f32x4 fold(f32x4 x, f32x4 y) { f32x4 r;
#pragma unroll
  for (int k = 0; k < 4; ++k) { auto s = __builtin_amdgcn_permlane16_swap(asu(x[k]), asu(y[k]), false, false); r[k] = asf(s[0]) + dpp<0x128>(asf(s[1])); }
  return r; }
DEV unsigned fold_slot(int lane) { return (lane >> 5) * 16 + 2 * (lane & 7) + ((lane >> 4) & 1); }
DEV float red8(f32x4 a) { float y = sw16add(sw32add(a[0], a[1]), sw32add(a[2], a[3])); return y + dpp<0x128>(y); }

DEV void wo_stage(const u16* wo, int row0, int cg, int w, int lane, unsigned t0) {
  const rsrc_t rw = mkrs(wo, 4096 * D * 2); const int gb = lane >> 4;
  const unsigned vo = (row0 + 2 * (gb >> 1) + 8 * (gb & 1) + 16 * ((lane >> 3) & 1)) * (D * 2) + cg * 128 + (lane & 7) * 16, la = L_WO + w * 16384 + lane * 16;
  v4u t[16]; bool done = false;
#pragma unroll
  for (int i = 0; i < 16 + LAGO; ++i) {
    if (i < 16) { if (i == NWO) lds_wait_slow(C_F, 9); if (i < NWO) pace(t0, TO0 + i * TOD, done); t[i] = ld16<AUX_NT>(rw, vo, (64 * (i >> 3) + 32 * ((i >> 1) & 1) + 4 * (i & 1) + ((i >> 2) & 1)) * (D * 2)); PIN(); }
    if (i >= LAGO) { const int k = i - LAGO; const unsigned a = la + (k >> 2) * 4096 + ((k >> 1) & 1) * 1024 + (k & 1) * 8;
      *lp<v2u>(a) = v2u{t[k][0], t[k][1]}; *lp<v2u>(a + 2048) = v2u{t[k][2], t[k][3]}; }
  }
  lds_inc(C_WO);
}
DEV void h_gather(rsrc_t ws, const u16* x, int cg, int lane, const Tag& g ST_PARAM) {
  const int bb = (lane >> 4) & 1, col = cg * 64 + 4 * (lane & 15) + 2 * (lane >> 5);
  const unsigned xv = __builtin_amdgcn_raw_buffer_load_b32(mkrs(x, 2 * D * 2), (bb * D + col) * 2, 0, 0);
  const unsigned ad = L_HI + bb * BST + (col / 18) * 80 + (col % 18) * 4, vo = (lane >> 5) * 4608 + (lane & 31) * 16;
  v4u u[4];
  sweep<4, 1>(ws, u, g, [&](int i, unsigned& v, unsigned& so) { v = vo; so = WS_OP + i * 9216 + cg * 512; });
  ST(7);
  const f32x4 a = (asf4(u[0]) + asf4(u[1])) + (asf4(u[2]) + asf4(u[3]));
  *lp<v2u>(ad) = v2u{cvtpk(0.f, sw32add(a[0], a[2]) + bflo(xv)), cvtpk(0.f, sw32add(a[1], a[3]) + bfhi(xv))};
  lds_inc(C_H);
}
DEV void down(const u16* w2, rsrc_t ws, int cb, int cg, int lane, const Tag& g ST_PARAM) {
  const rsrc_t rw = mkrs(w2, FF * D * 2); const int gb = lane >> 4;
  const unsigned vw = (cb * 64 + 8 * (gb >> 1) + 2 * (gb & 1) + 4 * ((lane >> 3) & 1)) * (D * 2) + cg * 128 + (lane & 7) * 16;
  const unsigned idx = (lane & 3) * 0x5555u, vo = fold_slot(lane) * 16;
  v4u w[8];
  lds_wait(C_Y, 9);
#pragma unroll
  for (int i = 0; i < 8; ++i) { if (i >= CAP2) vmw<CAP2 - 1>(); w[i] = ld16<AUX_NT>(rw, vw, (16 * (i >> 1) + (i & 1)) * (D * 2)); gap(); }
  const Quad q0 = regroup(w[0], w[1], w[2], w[3]), q1 = regroup(w[4], w[5], w[6], w[7]);
  ST(1);
  v4u u[2];
  sweep<2, 0>(ws, u, g, [&](int i, unsigned& v, unsigned& so) { v = lane * 16; so = WS_GU + cb * 2048 + i * 1024; });
  ST(2);
  v4u A[2];
#pragma unroll
  for (int i = 0; i < 2; ++i) { const float gt = asf(u[0][i]);
    A[i] = quad(hilo(gt * asf(u[1][i]) * __builtin_amdgcn_rcpf(1.f + __builtin_amdgcn_exp2f(gt * -1.4426950408889634f)))); }
  const f32x4 z = {0, 0, 0, 0}, r = fold(sm64(A[1], q1.x, sm64(A[0], q0.x, z, idx), idx), sm64(A[1], q1.y, sm64(A[0], q0.y, z, idx), idx));
  if (!(lane & 8)) publish(ws, vo, WS_DP + cb * 4608 + cg * 512, pack(r, g));
  ST(3);
}
DEV void final_sum(rsrc_t ws, u16* out, int ri, int k, int lane, const Tag& g ST_PARAM) {
  const unsigned vo = (lane >> 3) * 4608 + (lane & 7) * 16, ra = L_RP + k * 128 + (4 * (lane & 7) + (lane >> 5) + 2 * ((lane >> 4) & 1)) * 4;
  lds_wait_slow(C_HB, 1);
  if (RDELAY) __builtin_amdgcn_s_sleep(RDELAY);
  ST(6);
  v4u u[1];
  sweep<1, 0>(ws, u, g, [&](int i, unsigned& v, unsigned& so) { v = vo; so = WS_DP + 8 * k * 4608 + ri * 128; });
  ST(7);
  *lp<float>(ra) = red8(asf4(u[0]));
  if (lds_inc_rtn(C_R) == 8) {
    const int l = lane & 31; float y = *lp<float>(L_HB + l * 4);
#pragma unroll
    for (int i = 0; i < 9; ++i) y += *lp<float>(L_RP + i * 128 + l * 4);
    if (lane < 32) out[((ri >> 1) & 1) * D + (ri >> 2) * 64 + (ri & 1) * 32 + l] = (u16)cvtpk(y, y);
    if (ri == 0 && lane == 0) publish(ws, 0, 0, v4u{g.t[0], g.t[1], g.t[2], g.t[3]});
  }
  ST(8);
}

__global__ void __launch_bounds__(768, 3)
fused_decode_kernel(const u16* __restrict__ x, const u16* __restrict__ kc, const u16* __restrict__ vc, const u16* __restrict__ wq, const u16* __restrict__ wo,
                    const u16* __restrict__ w1, const u16* __restrict__ w3, const u16* __restrict__ w2, u16* __restrict__ out, unsigned* __restrict__ wsp) {
  __shared__ v4u lds_all[L_END / 16 + 1];
  asm volatile("" ::"s"((LDS v4u*)lds_all), "s"(x), "s"(kc), "s"(vc), "s"(wq), "s"(wo), "s"(w1), "s"(w3), "s"(w2), "s"(out), "s"(wsp));
  ST_DECL; ST(0);
  const int lane = threadIdx.x & 63, w = __builtin_amdgcn_readfirstlane(threadIdx.x >> 6), c = blockIdx.x;
  const int xg = c & 7, ic = c >> 3, kvh = 2 * xg + (ic >> 4), j = ic & 15, b = j >> 3, s = j & 7, cu = kvh * 16 + j;
  const bool isO = ic < 9, isF = !isO && ic < 27;
  const int fi = 18 * xg + ic - 9, ri = 5 * xg + ic - 27;
  const rsrc_t ws = mkrs(wsp, WS_END);
  const unsigned idx = (lane & 3) * 0x5555u;
  Tag g;
  if (w >= 4) {
    const int sw = w - 4;
    const rsrc_t rk = mkrs(kc, 2u * 16 * SK * 256), rv = mkrs(vc, 2u * 16 * SK * 256);
    const unsigned base = ((unsigned)(b * 16 + kvh) * SK + (SK - WIN) + s * 512 + sw * 64) * 256, rot = c * ROT_C + sw * ROT_W;
    const unsigned vk = base + ((lane >> 2) & 3) * 256 + (4 * (lane >> 4) + (lane & 3)) * 16, vv = base + lane * 16;
    constexpr float C2 = 0.08838834764831845f * 1.4426950408889634f;
    v4u K[NL], V[NL], qb; float sc[NL], mx = -INFINITY;
#pragma unroll
    for (int t = 0; t < NL; ++t) {
      if (t >= CAP) { if (CAPO != CAP && isO) vmw<CAPO - 1>(); else if (NLOW && sw < NLOW) vmw<CAP - 2>(); else vmw<CAP - 1>(); }
      K[t] = ld16<AUX_NT>(rk, vk, ((rot + t) & 15) * 1024);
      if (t < CAP - 1) gap();
      if (t == CAP - 1) { PIN(); asm volatile("s_barrier" ::: "memory"); PIN(); }
    }
    ST(1);
#pragma unroll
    for (int t = 0; t < NL; ++t) {
      if (t < VUN) { if (CAPO != CAP && isO) vmw<CAPO - 1>(); else if (NLOW && sw < NLOW) vmw<CAP - 2>(); else vmw<CAP - 1>(); } else gap();
      V[t] = ld16<AUX_NT>(rv, vv, ((rot + t) & 15) * 1024);
      PIN();
      if (t == QA) { lds_wait_slow(C_QF, 1); g = lds_tag(); qb = *lp<v4u>(L_Q + ((lane >> 3) & 1) * 256 + (4 * (lane >> 4) + (lane & 3)) * 16); ST(2); }
      if (t >= QA && t < QA + 8) {
#pragma unroll
        for (int k = 2 * (t - QA); k < 2 * (t - QA) + 2; ++k) {
          f32x4 d = mfma32(K[k], qb, f32x4{0, 0, 0, 0});
          sc[k] = (dpp<0x00>(d[0]) + dpp<0x55>(d[1]) + dpp<0xAA>(d[2]) + dpp<0xFF>(d[3])) * C2;
          mx = fmaxf(mx, sc[k]);
        }
      }
    }
    PIN();
    lds_inc(C_I);
    ST(3);
    mx = sw32max(sw16max(mx));
    const int hs = (lane >> 3) & 1, ha = lane >> 5;
    const unsigned mA = (lane & 4) ? 0u : ~0u;
    v2u A[NL]; float l = 0;
#pragma unroll
    for (int t = 0; t < NL; ++t) { float p = __builtin_amdgcn_exp2f(sc[t] - mx); l += p; unsigned cc = hilo(p); A[t] = v2u{cc & mA, cc & ~mA}; }
    l = sw16add(l, l); l = sw32add(l, l);
    *lp<float>(L_MX + hs * 32 + sw * 4) = mx; *lp<float>(L_L1 + hs * 32 + sw * 4) = l;
    lds_inc(C_M);
    f32x4 acc = {0, 0, 0, 0};
    constexpr int NPV = VUN - CAP;
#pragma unroll
    for (int t = 0; t < NPV; ++t) acc = sm32(A[t], V[t], acc, idx);
    PIN();
    lds_wait(C_M, 8);
    const float mi = *lp<float>(L_MX + ha * 32 + (lane & 7) * 4);
    float M = fmaxf(mi, dpp<0xB1>(mi)); M = fmaxf(M, dpp<0x4E>(M)); M = fmaxf(M, dpp<0x141>(M));
    float f = __builtin_amdgcn_exp2f(*lp<float>(L_MX + ha * 32 + sw * 4) - M);
    { float L = *lp<float>(L_L1 + ha * 32 + (lane & 7) * 4) * __builtin_amdgcn_exp2f(mi - M);
      L += dpp<0xB1>(L); L += dpp<0x4E>(L); L += dpp<0x141>(L);
      publish(ws, ((lane & 31) | sw) ? 0x80000000u : 1024 + ha * 16, WS_AP + cu * 1152, pack(f32x4{M, asf(asu(M) << 21), L, L}, g)); }
    asm volatile("" : "+v"(f)); PIN();
#pragma unroll
    for (int t = NPV; t < NL; ++t) acc = sm32(A[t], V[t], acc, idx);
    *lp<f32x4>(L_P1 + sw * 1024 + lane * 16) = acc * f;
    ST(4);
    if (lds_inc_rtn(C_F) == 7) {
      f32x4 o = *lp<f32x4>(L_P1 + lane * 16);
#pragma unroll
      for (int i = 1; i < 8; ++i) o += *lp<f32x4>(L_P1 + i * 1024 + lane * 16);
      publish(ws, (ha * 32 + 2 * (lane & 15) + ((lane >> 4) & 1)) * 16, WS_AP + cu * 1152, pack(o, g));
      lds_inc(C_F);
    }
    ST(5);
    if (isO) {
      lds_wait_slow(C_WO, 4);
      Quad q0, q1;
      { const unsigned a = L_WO + sw * 8192 + lane * 16;
        q0.x = __builtin_shufflevector(*lp<v4u>(a), *lp<v4u>(a + 1024), 0, 1, 2, 3, 4, 5, 6, 7); q0.y = __builtin_shufflevector(*lp<v4u>(a + 2048), *lp<v4u>(a + 3072), 0, 1, 2, 3, 4, 5, 6, 7);
        q1.x = __builtin_shufflevector(*lp<v4u>(a + 4096), *lp<v4u>(a + 5120), 0, 1, 2, 3, 4, 5, 6, 7); q1.y = __builtin_shufflevector(*lp<v4u>(a + 6144), *lp<v4u>(a + 7168), 0, 1, 2, 3, 4, 5, 6, 7);
        asm volatile("" : "+v"(q0.x), "+v"(q0.y), "+v"(q1.x), "+v"(q1.y)); }
      const unsigned pa = (((lane >> 3) & 1) * 8 + (lane >> 4)) * 1152, a0 = WS_AP + (2 * xg + (sw >> 2)) * 16 * 1152;
      const unsigned vd = pa + (sw & 3) * 256 + (lane & 7) * 16, vm = pa + (lane & 1) * 4 * 1152 + 1024 + ((sw >> 1) & 1) * 16;
      lds_wait(C_F, 9);
      if (ODELAY) __builtin_amdgcn_s_sleep(ODELAY);
      ST(6);
      v4u u[5];
      sweep<5, OSL>(ws, u, g, [&](int i, unsigned& v, unsigned& so) { v = i ? vd : vm; so = i ? a0 + ((i - 1) >> 1) * 4 * 1152 + ((i - 1) & 1) * 128 : a0; });
      ST(7);
      const float m = asf((u[0][0] & 0xfffff800u) | (u[0][1] >> 21));
      const float Mg = sw32max(sw16max(fmaxf(m, dpp<0xB1>(m)))), e = __builtin_amdgcn_exp2f(m - Mg);
      float L = e * asf(u[0][2]); L += dpp<0xB1>(L); L = sw16add(L, L); L = sw32add(L, L);
      const float fo = e * __builtin_amdgcn_rcpf(L), f0 = dpp<0x00>(fo), f1 = dpp<0x55>(fo);
      const f32x4 o0 = asf4(u[1]) * f0 + asf4(u[3]) * f1, o1 = asf4(u[2]) * f0 + asf4(u[4]) * f1;
      const float t0 = sw32add(o0[0], o1[0]), t1 = sw32add(o0[1], o1[1]), t2 = sw32add(o0[2], o1[2]), t3 = sw32add(o0[3], o1[3]);
      const v4u A0 = quad(hilo(sw16add(t0, t2))), A1 = quad(hilo(sw16add(t1, t3)));
      const f32x4 z = {0, 0, 0, 0}, r = fold(sm64(A1, q1.x, sm64(A0, q0.x, z, idx), idx), sm64(A1, q1.y, sm64(A0, q0.y, z, idx), idx));
      if (!(lane & 8)) *lp<f32x4>(L_YP + sw * 512 + fold_slot(lane) * 16) = r;
      if (lds_inc_rtn(C_Y) == 7) {
        const unsigned ya = L_YP + lane * 16;
        const f32x4 y = (*lp<f32x4>(ya) + *lp<f32x4>(ya + 1024)) + (*lp<f32x4>(ya + 2048) + *lp<f32x4>(ya + 3072));
        const f32x4 yy = {sw32add(y[0], y[0]), sw32add(y[1], y[1]), sw32add(y[2], y[2]), sw32add(y[3], y[3])};
        if (lane < 32) publish(ws, lane * 16, WS_OP + xg * 4608 + ic * 512, pack(yy, g));
        lds_inc(C_Y);
      }
      ST(8);
      down(w2, ws, 9 * xg + ic, sw, lane, g ST_ARG);
    } else if (isF) {
      f32x2 cs = {0, 0};
      const unsigned uo = (8 * (lane >> 4) + (lane & 7)) * 4;
      if (sw == 0) {
        lds_wait_slow(C_W, 4);
#pragma unroll
        for (int a = 0; a < 4; ++a) { cs[0] += *lp<float>(L_CS + uo + a * 256); cs[1] += *lp<float>(L_CS + uo + a * 256 + 128); }
        asm volatile("" : "+v"(cs));
      } else if (HG) lds_wait_slow(HG == 1 ? C_WI : C_W, 4);
      ST(6);
      h_gather(ws, x, sw, lane, g ST_ARG);
      if (sw == 0) {
        const unsigned ha2 = L_HI + (lane >> 5) * BST + (lane & 31) * 80;
        lds_wait(C_H, 9);
        f32x4 hv[4]; f32x2 ht = *lp<f32x2>(ha2 + 64);
#pragma unroll
        for (int i = 0; i < 4; ++i) hv[i] = *lp<f32x4>(ha2 + i * 16);
        const f32x4 sv = (hv[0] + hv[1]) + (hv[2] + hv[3]);
        const float m = sum32((sv[0] + sv[1]) + (sv[2] + sv[3]) + (ht[0] + ht[1])) * (1.f / D);
        f32x4 q = {0, 0, 0, 0};
#pragma unroll
        for (int i = 0; i < 4; ++i) { f32x4 d = hv[i] - m; q += d * d; }
        ht -= m; ht *= ht;
        const float rs = __builtin_amdgcn_rsqf(sum32((q[0] + q[1]) + (q[2] + q[3]) + (ht[0] + ht[1])) * (1.f / D) + 1e-5f);
        const auto ms = __builtin_amdgcn_permlane32_swap(asu(m), asu(m), false, false), rr = __builtin_amdgcn_permlane32_swap(asu(rs), asu(rs), false, false);
        const float mb = asf((lane & 8) ? ms[1] : ms[0]), rb = asf((lane & 8) ? rr[1] : rr[0]);
        const unsigned ga = L_GP + ((lane >> 3) & 1) * 256 + uo;
        f32x2 y = -mb * cs;
        asm volatile("" : "+v"(y));
        lds_wait(C_G, 4);
#pragma unroll
        for (int a = 0; a < 4; ++a) { y[0] += *lp<float>(ga + a * 512); y[1] += *lp<float>(ga + a * 512 + 128); }
        y *= rb;
        publish(ws, lane * 16, WS_GU + fi * 1024, pack(f32x4{y[0], y[1], y[0], y[1]}, g));
      }
      ST(8);
    } else if (ri < 36) final_sum(ws, out, ri, sw, lane, g ST_ARG);
  } else {
    __builtin_amdgcn_s_setprio(3);
    const unsigned t0 = __builtin_amdgcn_s_memrealtime();
    if (w == 0) { if (lane < 12) *lp<unsigned>(lane * 4) = 0; asm volatile("s_waitcnt lgkmcnt(0)" ::: "memory"); }
    {
      const rsrc_t rx = mkrs(x, 2 * D * 2), rq = mkrs(wq, D * 4096 * 2);
      const int r0 = 144 * (j >> 2) + 36 * w, gp = (lane >> 3), ap = 2 * (lane >> 4) + ((lane >> 3) & 1);
      const unsigned cq = (kvh * 256 + (j & 3) * 64 + (lane & 7) * 8) * 2, vq = (r0 + 4 * gp) * 8192 + cq, vt = gp < 4 ? (r0 + 32 + gp) * 8192 + cq : 0x80000000u;
      v4u wv[5];
#pragma unroll
      for (int i = 0; i < 4; ++i) wv[i] = ld16<AUX_NT>(rq, vq, i * 8192);
      wv[4] = ld16<AUX_NT>(rq, vt, 0);
      const v4u ep = ld16<AUX_SC1>(ws, 0, 0);
      const unsigned xo = (lane >> 5) * (D * 2);
      const v4u xs0 = ld16<0>(rx, xo + (lane & 31) * 16, 0), xs1 = ld16<0>(rx, xo + (lane & 31) * 16, 512);
      const unsigned xs2 = __builtin_amdgcn_raw_buffer_load_b32(rx, xo + (lane & 31) * 4, 1024, 0);
      v2u xa[2]; unsigned xt[2];
#pragma unroll
      for (int bb = 0; bb < 2; ++bb) { xa[bb] = __builtin_amdgcn_raw_buffer_load_b64(rx, (r0 + 4 * ap) * 2, bb * D * 2, 0);
        xt[bb] = __builtin_amdgcn_raw_buffer_load_b16(rx, (r0 + 32 + (ap & 3)) * 2, bb * D * 2, 0); }
      PIN(); asm volatile("s_barrier" ::: "memory"); PIN();
      const unsigned xs[9] = {xs0[0], xs0[1], xs0[2], xs0[3], xs1[0], xs1[1], xs1[2], xs1[3], xs2};
      float sm = 0;
#pragma unroll
      for (int k = 0; k < 9; ++k) sm += bflo(xs[k]) + bfhi(xs[k]);
      const float m = sum32(sm) * (1.f / D); float q = 0;
#pragma unroll
      for (int k = 0; k < 9; ++k) { float d0 = bflo(xs[k]) - m, d1 = bfhi(xs[k]) - m; q += d0 * d0 + d1 * d1; }
      const float r = __builtin_amdgcn_rsqf(sum32(q) * (1.f / D) + 1e-5f);
      const unsigned mA = (lane & 4) ? 0u : ~0u;
      f32x4 a[2];
#pragma unroll
      for (int bb = 0; bb < 2; ++bb) {
        const float mm = asf(__builtin_amdgcn_readlane(asu(m), 32 * bb)), rr = asf(__builtin_amdgcn_readlane(asu(r), 32 * bb));
        const float xv[5] = {bflo(xa[bb][0]), bfhi(xa[bb][0]), bflo(xa[bb][1]), bfhi(xa[bb][1]), bflo(xt[bb])};
        a[bb] = f32x4{0, 0, 0, 0};
#pragma unroll
        for (int t = 0; t < 5; ++t) { unsigned cc = hilo((xv[t] - mm) * rr); a[bb] = sm32(v2u{cc & mA, cc & ~mA}, wv[t], a[bb], idx); }
      }
      f32x4 qp;
#pragma unroll
      for (int k = 0; k < 4; ++k) { auto sp = __builtin_amdgcn_permlane32_swap(asu(a[0][k]), asu(a[1][k]), false, false); qp[k] = asf(sp[0]) + dpp<0x128>(asf(sp[1])); }
      if (!(lane & 8)) *lp<f32x4>(L_QP + w * 512 + fold_slot(lane) * 16) = qp;
      ST(1);
      if (lds_inc_rtn(C_Q) == 3) {
#pragma unroll
        for (int i = 0; i < 4; ++i) g.t[i] = ((__builtin_amdgcn_readfirstlane(ep[i]) + 1) & 0x3ffu) | 0x400u;
        const f32x4 y = *lp<f32x4>(L_QP + lane * 16) + *lp<f32x4>(L_QP + 1024 + lane * 16);
        const f32x4 yy = {sw32add(y[0], y[0]), sw32add(y[1], y[1]), sw32add(y[2], y[2]), sw32add(y[3], y[3])};
        if (lane < 32) publish(ws, (lane >> 4) * 4096 + (lane & 15) * 16, WS_QP + kvh * 8192 + (j >> 2) * 1024 + (j & 3) * 256, pack(yy, g));
        __builtin_amdgcn_s_sleep(16);
        v4u u[4];
        sweep<4, 8>(ws, u, g, [&](int i, unsigned& v, unsigned& so) { v = lane * 16; so = WS_QP + kvh * 8192 + b * 4096 + i * 1024; });
        const f32x4 qq = (asf4(u[0]) + asf4(u[1])) + (asf4(u[2]) + asf4(u[3]));
        *lp<v2u>(L_Q + lane * 8) = v2u{cvtpk(qq[0], qq[1]), cvtpk(qq[2], qq[3])};
        *lp<v4u>(L_TAG) = v4u{g.t[0], g.t[1], g.t[2], g.t[3]};
        lds_inc(C_QF);
        ST(2);
      }
    }
    __builtin_amdgcn_s_setprio(0);
    if (isF) {
      const int gb = lane >> 4, par = (lane >> 3) & 1;
      const GLB char* wb = (const GLB char*)((fi & 1) ? w3 : w1) + (size_t)(144 * w + 18 * par) * (FF * 2) + (fi >> 1) * 128 + (lane & 7) * 16;
      const GLB char* wp = wb + (36 * (gb >> 1) + 2 * (gb & 1)) * (FF * 2); const GLB char* wl = wb + (36 * gb + 16) * (FF * 2);
      v4u wv[18]; bool done = false;
      ST(3);
#pragma unroll
      for (int i = 0; i < 18; ++i) {
        pace(t0, TF0 + i * TFD, done);
        if (i >= CAPW) vmw<CAPW - 1>();
        wv[i] = __builtin_nontemporal_load((const GLB v4u*)(i < 16 ? wp + (72 * ((i >> 1) & 1) + (i & 1) + 4 * (i >> 2)) * (FF * 2) : wl + (i - 16) * (FF * 2)));
        gap();
      }
      PIN();
      lds_inc(C_WI);
      ST(4);
      lds_wait_slow(C_QF, 1);
      g = lds_tag();
      Quad wg[4];
#pragma unroll
      for (int i = 0; i < 4; ++i) wg[i] = regroup(wv[4 * i], wv[4 * i + 1], wv[4 * i + 2], wv[4 * i + 3]);
      v4u tx = {wv[16][0], wv[16][1], wv[17][0], wv[17][1]}, ty = {wv[16][2], wv[16][3], wv[17][2], wv[17][3]};
      asm volatile("" : "+v"(tx), "+v"(ty));
      const unsigned gw = (lane & 7) * 32 + ((lane >> 4) & 1) * 16;
      { const v4u one = {0x3f80u, 0x3f80u, 0x3f80u, 0x3f80u};
        f32x4 cx = sm32(v2u{0x3f80u, 0x3f80u}, tx, f32x4{0, 0, 0, 0}, idx), cy = sm32(v2u{0x3f80u, 0x3f80u}, ty, f32x4{0, 0, 0, 0}, idx);
#pragma unroll
        for (int i = 0; i < 4; ++i) { cx = sm64(one, wg[i].x, cx, idx); cy = sm64(one, wg[i].y, cy, idx); }
        const f32x4 r = fold(cx, cy);
        if (!(lane & 40)) *lp<f32x4>(L_CS + w * 256 + gw) = r;
        lds_inc(C_W); }
      ST(5);
      if (w == 0) { ST(6); h_gather(ws, x, 8, lane, g ST_ARG); }
      const unsigned ia = L_HI + par * BST + (8 * w + 2 * gb + ((lane >> 2) & 1)) * 80;
      if (w) lds_wait_slow(C_H, 1);
      lds_wait(C_H, 9);
      if (w) ST(7);
      const v2u At = *lp<v2u>(ia + 64);
      f32x4 ax = sm32(At, tx, f32x4{0, 0, 0, 0}, idx), ay = sm32(At, ty, f32x4{0, 0, 0, 0}, idx);
#pragma unroll
      for (int i = 0; i < 4; ++i) { const v4u Ai = *lp<v4u>(ia + i * 16); ax = sm64(Ai, wg[i].x, ax, idx); ay = sm64(Ai, wg[i].y, ay, idx); }
      const f32x4 r = fold(ax, ay);
      if (!(lane & 8)) *lp<f32x4>(L_GP + w * 512 + (lane >> 5) * 256 + gw) = r;
      lds_inc(C_G);
      ST(8);
    } else if (isO) { ST(3); wo_stage(wo, 512 * xg + 128 * w, ic, w, lane, t0); ST(5);
      if (w == 0) { lds_wait_slow(C_F, 8); g = lds_tag(); lds_wait_slow(C_Y, 1); down(w2, ws, 9 * xg + ic, 8, lane, g ST_ARG); }
    } else if (ri < 36 && w < 2) {
      lds_wait_slow(C_F, 8); g = lds_tag();
      if (w == 0) final_sum(ws, out, ri, 8, lane, g ST_ARG);
      else {
        const int cl = 4 * (lane & 7) + (lane >> 5) + 2 * ((lane >> 4) & 1);
        const float xv = bflo(x[((ri >> 1) & 1) * D + (ri >> 2) * 64 + (ri & 1) * 32 + cl]);
        v4u u[1];
        sweep<1, 2>(ws, u, g, [&](int i, unsigned& v, unsigned& so) { v = (lane >> 3) * 4608 + (lane & 7) * 16; so = WS_OP + ri * 128; });
        *lp<unsigned>(L_HB + cl * 4) = cvtpk(0.f, red8(asf4(u[0])) + xv);
        lds_inc(C_HB); ST(7);
      }
    }
  }
}

extern "C" void kernel_launch(void* const* d_in, const int* in_sizes, int n_in, void* d_out, int out_size, void* d_ws, size_t ws_size, hipStream_t stream) {
  fused_decode_kernel<<<256, 768, 0, stream>>>((const u16*)d_in[0], (const u16*)d_in[1], (const u16*)d_in[2], (const u16*)d_in[3], (const u16*)d_in[4],
                                               (const u16*)d_in[6], (const u16*)d_in[7], (const u16*)d_in[8], (u16*)d_out, (unsigned*)d_ws);
}
```

```cpp
#include <hip/hip_runtime.h>
#include <hip/hip_bf16.h>
#include <stdint.h>

typedef unsigned v2u __attribute__((ext_vector_type(2)));
typedef unsigned v4u __attribute__((ext_vector_type(4)));
typedef unsigned v8u __attribute__((ext_vector_type(8)));
typedef unsigned v16u __attribute__((ext_vector_type(16)));
typedef float f32x2 __attribute__((ext_vector_type(2)));
typedef float f32x4 __attribute__((ext_vector_type(4)));
typedef short s16x4 __attribute__((ext_vector_type(4)));
typedef short s16x8 __attribute__((ext_vector_type(8)));
typedef __bf16 bf16x2_t __attribute__((ext_vector_type(2)));
typedef __bf16 bf16x8_t __attribute__((ext_vector_type(8)));
typedef __bf16 bf16x16_t __attribute__((ext_vector_type(16)));
typedef unsigned short u16;
typedef __amdgpu_buffer_rsrc_t rsrc_t;
#define LDS __attribute__((address_space(3)))
#define DEV __device__ __forceinline__
#define PIN() __builtin_amdgcn_sched_barrier(0)

constexpr int D = 576, SK = 32768, WIN = 4096, FF = 4608, NK = 17, NKO = 9;
constexpr int AUX_NT = 2, AUX_SC1 = 16;
#define ROT_C 5
#define ROT_W 15
#define CAP 3
#define CAPO 3
#define QA 6
#define QAO 2
#define CAPA 2
#define GATE C_I
#define GATEN 8
#define GATED C_F
#define GATEDN 9
#define CSSL 4
#define HSL 1
#define PT 1
#define TW 1400
#define TD 1450
#define DW 50
#define DD 50
#define AD 1
#define CAP2 4
#define AD_A 50
#define AD_B 230
#define AE_A 0
#define AE_B 236
#define AE_0 6
#define AD_0 3
#define FSL 0
#define DSL 0
#define TSG 0
#define TSD 0
#define BURSTN 8
#define STR_(x) #x
#define STR(x) STR_(x)
#define PRIO 0
constexpr unsigned WS_QP = 4096;
constexpr unsigned WS_AP = WS_QP + 16 * 8192;
constexpr unsigned WS_OP = WS_AP + 256 * 1152;
constexpr unsigned WS_GU = WS_OP + 9 * 16384;
constexpr unsigned WS_CS = WS_GU + 72 * 9216;
constexpr unsigned WS_ST = WS_CS + 72 * 4608;
constexpr unsigned WS_DP = WS_ST + 72 * 128;
constexpr unsigned WS_END = WS_DP + 72 * 4608;
constexpr unsigned C_Q = 0, C_QF = 4, C_I = 8, C_M = 12, C_F = 16, C_A = 20, C_CS = 24, C_WI = 28, C_R = 32, C_HB = 36;
constexpr unsigned L_TAG = 48;
constexpr unsigned L_ST = 64;
constexpr unsigned L_MX = 128;
constexpr unsigned L_L1 = 192;
constexpr unsigned L_Q = 256;
constexpr unsigned L_QP = 768;
constexpr unsigned L_P1 = L_QP + 2048;
constexpr unsigned L_A = L_P1 + 8192;
constexpr unsigned L_CS = L_A + 5120;
constexpr unsigned L_HB = L_CS + 512;
constexpr unsigned L_RP = L_HB + 512;
constexpr unsigned L_END = L_RP + 1152;
#define LALL (96 * 1024)
constexpr unsigned L_ALL = LALL;
constexpr unsigned CODE_BYTES = 20 * 1024;
constexpr unsigned SPIN_G = 1u << 18, SPIN_L = 1u << 21;


DEV float asf(unsigned u) { return __builtin_bit_cast(float, u); }
DEV unsigned asu(float f) { return __builtin_bit_cast(unsigned, f); }
DEV float bflo(unsigned w) { return asf(w << 16); }
DEV float bfhi(unsigned w) { return asf(w & 0xffff0000u); }
DEV unsigned cvtpk(float lo, float hi) { return __builtin_bit_cast(unsigned, __builtin_convertvector(f32x2{lo, hi}, bf16x2_t)); }
DEV unsigned hilo(float v) { float h = asf(asu(v) & 0xffff0000u); return cvtpk(h, v - h); }
DEV f32x4 mfma32(v4u a, v4u b, f32x4 c) {
  return __builtin_amdgcn_mfma_f32_16x16x32_bf16(__builtin_bit_cast(bf16x8_t, a), __builtin_bit_cast(bf16x8_t, b), c, 0, 0, 0);
}
DEV f32x4 sm32(v2u a, v4u b, f32x4 c, unsigned idx) {
  return __builtin_amdgcn_smfmac_f32_16x16x32_bf16(__builtin_bit_cast(s16x4, a), __builtin_bit_cast(s16x8, b), c, idx, 0, 0);
}
DEV f32x4 sm64(v4u a, v8u b, f32x4 c, unsigned idx) {
  return __builtin_amdgcn_smfmac_f32_16x16x64_bf16(__builtin_bit_cast(bf16x8_t, a), __builtin_bit_cast(bf16x16_t, b), c, idx, 0, 0);
}
template <int CTRL> DEV unsigned dppu(unsigned x) { return __builtin_amdgcn_mov_dpp(x, CTRL, 0xf, 0xf, true); }
template <int CTRL> DEV float dpp(float x) { return asf(dppu<CTRL>(asu(x))); }
DEV v4u quad(unsigned c) { return v4u{dppu<0x00>(c), dppu<0x55>(c), dppu<0xAA>(c), dppu<0xFF>(c)}; }
DEV float sw32add(float a, float b) { auto r = __builtin_amdgcn_permlane32_swap(asu(a), asu(b), false, false); return asf(r[0]) + asf(r[1]); }
DEV float sw16add(float a, float b) { auto r = __builtin_amdgcn_permlane16_swap(asu(a), asu(b), false, false); return asf(r[0]) + asf(r[1]); }
DEV float sw32max(float a) { auto r = __builtin_amdgcn_permlane32_swap(asu(a), asu(a), false, false); return fmaxf(asf(r[0]), asf(r[1])); }
DEV float sw16max(float a) { auto r = __builtin_amdgcn_permlane16_swap(asu(a), asu(a), false, false); return fmaxf(asf(r[0]), asf(r[1])); }
DEV float sum32(float v) {
  v += dpp<0xB1>(v); v += dpp<0x4E>(v); v += dpp<0x124>(v); v += dpp<0x128>(v); return sw16add(v, v);
}
DEV rsrc_t mkrs(const void* p, unsigned n) { return __builtin_amdgcn_make_buffer_rsrc((void*)p, 0, n, 0x00020000); }
template <int AUX> DEV v4u ld16(rsrc_t r, unsigned vo, unsigned so) { return __builtin_amdgcn_raw_buffer_load_b128(r, vo, so, AUX); }
template <int N> DEV void vmw() { PIN(); __builtin_amdgcn_s_waitcnt((N & 15) | 0x0f70 | ((N >> 4) << 14)); PIN(); }
constexpr int inflight(int a, int b) { int n = 0; for (int i = a; i < b; ++i) n += i < 4 ? 4 : 2; return n; }
DEV void vmw_n(int n) { if (n == 0) vmw<0>(); if (n == 2) vmw<2>(); if (n == 4) vmw<4>(); if (n == 6) vmw<6>(); if (n == 8) vmw<8>(); if (n == 10) vmw<10>(); if (n == 12) vmw<12>(); }
DEV void gap() { PIN(); asm volatile("s_nop 7"); PIN(); }
DEV unsigned now() { return (unsigned)__builtin_amdgcn_s_memrealtime(); }
template <int C, int C2, int T, int TE> DEV void room(unsigned t0) { PIN();
#define CLK "s_memrealtime vcc\n\ts_waitcnt lgkmcnt(0)\n\ts_sub_u32 vcc_lo, vcc_lo, %0\n\t"
  if (TE > 0) asm volatile(CLK "s_cmp_gt_u32 vcc_lo, %1\n\ts_cbranch_scc1 .Llate%=\n\ts_waitcnt vmcnt(%2)\n\ts_cmp_ge_u32 vcc_lo, %4\n\ts_cbranch_scc1 .Llate%=\n"
               ".Learly%=:\n\t" CLK "s_cmp_lt_u32 vcc_lo, %4\n\ts_cbranch_scc1 .Learly%=\n.Llate%=:\n\ts_waitcnt vmcnt(%3)"
               ::"s"(t0), "n"(T), "n"(C - 1), "n"(C2 - 1), "n"(TE) : "memory", "scc", "vcc");
  else asm volatile(CLK "s_cmp_gt_u32 vcc_lo, %1\n\ts_cbranch_scc1 .Llate%=\n\ts_waitcnt vmcnt(%2)\n.Llate%=:\n\ts_waitcnt vmcnt(%3)"
               ::"s"(t0), "n"(T), "n"(C - 1), "n"(C2 - 1) : "memory", "scc", "vcc");
  PIN(); }
template <int C, int I> DEV void room_at(int t, unsigned t0) { if constexpr (I < 40) { if (t == I) { if (AD && I >= AD_0) room<C, CAP2, AD_A + I * AD_B / 8, AE_B && I >= AE_0 ? AE_A + I * AE_B / 8 : 0>(t0); else vmw<C - 1>(); } else room_at<C, I + 1>(t, t0); } }
DEV f32x4 asf4(v4u u) { return __builtin_bit_cast(f32x4, u); }

struct Tag { unsigned t[4], m; };
DEV v4u pack(f32x4 v, const Tag& g) { return v4u{(asu(v[0]) & g.m) | g.t[0], (asu(v[1]) & g.m) | g.t[1], (asu(v[2]) & g.m) | g.t[2], (asu(v[3]) & g.m) | g.t[3]}; }
DEV void publish(rsrc_t ws, unsigned vo, unsigned so, v4u d) { __builtin_amdgcn_raw_buffer_store_b128(d, ws, vo, so, AUX_SC1); asm volatile("s_nop 1" ::"v"(d) : "memory"); }
#define MLD(i) "s_mov_b64 exec, %[e" #i "]\n\tbuffer_load_dwordx4 %[u" #i "], %[v" #i "], %[ws], %[s" #i "] offen sc1\n\t"
#define MLO_(i) [u##i] "+v"(u[i])
#define MLI(i) [e##i] "s"(bad[i]), [v##i] "v"(vo[i]), [s##i] "s"(so[i])
template <int N, int SLEEP, typename FO> DEV void sweep(rsrc_t ws, f32x4 (&x)[N], const Tag& g, FO off) {
  unsigned long long bad[N]; v4u u[N]; unsigned vo[N], so[N];
#pragma unroll
  for (int i = 0; i < N; ++i) { bad[i] = ~0ull; off(i, vo[i], so[i]); asm volatile("" : "=v"(u[i])); }
#pragma unroll 1
  for (unsigned spin = 0; spin < SPIN_G; ++spin) {
    if constexpr (N == 1) asm volatile(MLD(0) "s_mov_b64 exec, -1\n\ts_waitcnt vmcnt(0)" : MLO_(0) : MLI(0), [ws] "s"(ws) : "memory");
    if constexpr (N == 4) asm volatile(MLD(0) MLD(1) MLD(2) MLD(3) "s_mov_b64 exec, -1\n\ts_waitcnt vmcnt(0)" : MLO_(0), MLO_(1), MLO_(2), MLO_(3) : MLI(0), MLI(1), MLI(2), MLI(3), [ws] "s"(ws) : "memory");
    if constexpr (N == 5) asm volatile(MLD(0) MLD(1) MLD(2) MLD(3) MLD(4) "s_mov_b64 exec, -1\n\ts_waitcnt vmcnt(0)" : MLO_(0), MLO_(1), MLO_(2), MLO_(3), MLO_(4)
                                       : MLI(0), MLI(1), MLI(2), MLI(3), MLI(4), [ws] "s"(ws) : "memory");
    unsigned long long any = 0;
#pragma unroll
    for (int i = 0; i < N; ++i) { const v4u y = {u[i][0] ^ g.t[0], u[i][1] ^ g.t[1], u[i][2] ^ g.t[2], u[i][3] ^ g.t[3]};
      x[i] = asf4(y); bad[i] = __builtin_amdgcn_ballot_w64(((y[0] | y[1] | y[2] | y[3]) & 0x7ffu) != 0); any |= bad[i]; }
    if (!any) break;
    if (SLEEP) __builtin_amdgcn_s_sleep(SLEEP);
  }
}

DEV void touch(rsrc_t ws, unsigned a, unsigned b, unsigned so) { unsigned d0, d1;
  asm volatile("buffer_load_dword %0, %2, %4, %5 offen sc1\n\tbuffer_load_dword %1, %3, %4, %5 offen sc1\n\ts_waitcnt vmcnt(0)" : "=&v"(d0), "=&v"(d1) : "v"(a), "v"(b), "s"(ws), "s"(so) : "memory"); }

template <typename T> DEV LDS T* lp(unsigned off) { return (LDS T*)(uintptr_t)off; }
DEV void lds_inc(unsigned off) { asm volatile("s_mov_b64 exec, 1\n\tds_add_u32 %0, %1\n\ts_mov_b64 exec, -1" ::"v"(off), "v"(1u) : "memory"); }
DEV unsigned lds_inc_rtn(unsigned off) { unsigned o;
  asm volatile("s_mov_b64 exec, 1\n\tds_add_rtn_u32 %0, %1, %2\n\ts_mov_b64 exec, -1\n\ts_waitcnt lgkmcnt(0)" : "=v"(o) : "v"(off), "v"(1u) : "memory");
  return __builtin_amdgcn_readfirstlane(o); }
template <int SLEEP> DEV void lds_spin(unsigned off, unsigned val) { unsigned v, n;
  asm volatile("s_mov_b32 %1, %4\n"
               ".Lspin%=:\n\tds_read_b32 %0, %2\n\ts_sub_u32 %1, %1, 1\n\ts_waitcnt lgkmcnt(0)\n\ts_cbranch_scc1 .Ldone%=\n\t"
               "v_cmp_gt_u32 vcc, %3, %0\n\ts_cbranch_vccz .Ldone%=\n\ts_sleep %5\n\ts_branch .Lspin%=\n"
               ".Ldone%=:" : "=&v"(v), "=&s"(n) : "v"(off), "s"(val), "n"(SPIN_L), "n"(SLEEP) : "memory", "vcc", "scc"); }
DEV void lds_wait(unsigned off, unsigned val) { lds_spin<0>(off, val); }
DEV void lds_wait_slow(unsigned off, unsigned val) { lds_spin<2>(off, val); }
DEV void bar() { asm volatile("s_waitcnt lgkmcnt(0)\n\ts_barrier" ::: "memory"); }
DEV unsigned vmask() { unsigned m = 0xfffff800u; asm volatile("" : "+v"(m)); return m; }
DEV Tag lds_tag() { v4u t = *lp<v4u>(L_TAG); Tag g;
#pragma unroll
  for (int i = 0; i < 4; ++i) g.t[i] = __builtin_amdgcn_readfirstlane(t[i]);
  g.m = vmask(); return g; }

struct Quad { v8u x, y; };
DEV Quad regroup(v4u a, v4u b, v4u c, v4u d) {
  Quad q; q.x = v8u{a[0], a[1], b[0], b[1], c[0], c[1], d[0], d[1]}; q.y = v8u{a[2], a[3], b[2], b[3], c[2], c[3], d[2], d[3]};
  asm volatile("" : "+v"(q.x), "+v"(q.y)); return q;
}
DEV unsigned sel(bool c, unsigned a, unsigned b) { return c ? a : b; }
DEV unsigned lo32(int lane, unsigned v) { return v | (unsigned)(lane & 32) << 26; }
DEV int wrow(int lane) { return 8 * (lane >> 5) + 2 * ((lane >> 4) & 1) + 4 * ((lane >> 3) & 1); }
DEV int alane(int row, int b) { return 16 * (row >> 3) + 8 * b + (row & 7); }
DEV f32x4 fold(f32x4 x, f32x4 y) { f32x4 r;
#pragma unroll
  for (int k = 0; k < 4; ++k) { auto s = __builtin_amdgcn_permlane16_swap(asu(x[k]), asu(y[k]), false, false); r[k] = asf(s[0]) + dpp<0x128>(asf(s[1])); }
  return r; }
DEV unsigned fold_off(int lane) { return ((lane >> 5) * 16 + 2 * (lane & 7) + ((lane >> 4) & 1)) * 16 | (unsigned)(lane & 8) << 28; }
DEV float red8(f32x4 a) { float y = sw16add(sw32add(a[0], a[1]), sw32add(a[2], a[3])); return y + dpp<0x128>(y); }
#define DPPM " row_mask:0xf bank_mask:"
#define QP(i) " quad_perm:[" #i "," #i "," #i "," #i "]" DPPM
#define QUAD(a, b, c, d, s) "v_mov_b32_dpp v" #a ", v" #s QP(0) "0xf\n\tv_mov_b32_dpp v" #b ", v" #s QP(1) "0xf\n\tv_mov_b32_dpp v" #c ", v" #s QP(2) "0xf\n\tv_mov_b32_dpp v" #d ", v" #s QP(3) "0xf\n\t"
#define SM(d, a, b) "v_smfmac_f32_16x16x64_bf16 v[" d "], v[" a "], %[" b "], %[idx]\n\t"
#define ZERO4(a, b) "v_mov_b64 v[" a "], 0\n\tv_mov_b64 v[" b "], 0\n\t"
#define PKA(d, a, b) "v_pk_add_f32 v[" d "], v[" a "], v[" b "]\n\t"
#define FOLD1(x, y) "v_add_f32_dpp v" #x ", v" #y ", v" #x " row_ror:8" DPPM "0xf\n\t"
#define FOLD(a, b, c, d, e, f, g, h) "v_permlane16_swap_b32 v" #a ", v" #e "\n\tv_permlane16_swap_b32 v" #b ", v" #f "\n\tv_permlane16_swap_b32 v" #c ", v" #g "\n\tv_permlane16_swap_b32 v" #d ", v" #h "\n\t" \
  FOLD1(a, e) FOLD1(b, f) FOLD1(c, g) FOLD1(d, h)
#define PACK(a, b, c, d) "v_and_or_b32 v" #a ", v" #a ", %[m], %[t0]\n\tv_and_or_b32 v" #b ", v" #b ", %[m], %[t1]\n\tv_and_or_b32 v" #c ", v" #c ", %[m], %[t2]\n\tv_and_or_b32 v" #d ", v" #d ", %[m], %[t3]\n\t"
#define UNTAG(a, b, c, d, e, f, g, h) "v_xor_b32 v" #a ", %[t0], v" #e "\n\tv_xor_b32 v" #b ", %[t1], v" #f "\n\tv_xor_b32 v" #c ", %[t2], v" #g "\n\tv_xor_b32 v" #d ", %[t3], v" #h "\n\t"
#define LGKM(n) "s_waitcnt lgkmcnt(" #n ")\n\t"
#define TAGS [m] "v"(g.m), [t0] "s"(g.t[0]), [t1] "s"(g.t[1]), [t2] "s"(g.t[2]), [t3] "s"(g.t[3])
#define V8(a, b, c, d, e, f, g, h) "v" #a, "v" #b, "v" #c, "v" #d, "v" #e, "v" #f, "v" #g, "v" #h
#define V100 V8(100, 101, 102, 103, 104, 105, 106, 107), V8(108, 109, 110, 111, 112, 113, 114, 115), V8(116, 117, 118, 119, 120, 121, 122, 123)
#define V124 V8(124, 125, 126, 127, 128, 129, 130, 131), V8(132, 133, 134, 135, 136, 137, 138, 139), V8(140, 141, 142, 143, 144, 145, 146, 147)
#define RETRY2(sleep) ".Lre%=:\n\tv_or3_b32 v116, v108, v109, v110\n\tv_or3_b32 v117, v112, v113, v114\n\tv_bitop3_b32 v116, v116, %[k], v111 bitop3:0xc8\n\tv_bitop3_b32 v117, v117, %[k], v115 bitop3:0xc8\n\t" \
  "v_cmp_ne_u32 %[ea], 0, v116\n\tv_cmp_ne_u32 %[eb], 0, v117\n\ts_sub_u32 %[n], %[n], 1\n\ts_cbranch_scc1 .Lout%=\n\t" sleep \
  ".Lgo%=:\n\ts_mov_b64 exec, %[ea]\n\tbuffer_load_dwordx4 v[100:103], %[vd], %[ws], %[sa] offen sc1\n\ts_mov_b64 exec, %[eb]\n\tbuffer_load_dwordx4 v[104:107], %[vd], %[ws], %[sb] offen sc1\n\ts_mov_b64 exec, -1\n" \
  ".Lchk%=:\n\ts_waitcnt vmcnt(0)\n\t" UNTAG(108, 109, 110, 111, 100, 101, 102, 103) UNTAG(112, 113, 114, 115, 104, 105, 106, 107) \
  "v_or3_b32 v116, v108, v109, v110\n\tv_or3_b32 v117, v111, v112, v113\n\tv_or3_b32 v116, v116, v114, v115\n\tv_bitop3_b32 v116, v116, %[k], v117 bitop3:0xc8\n\t" \
  "v_cmp_ne_u32 vcc, 0, v116\n\ts_cbranch_vccnz .Lre%=\n.Lout%=:\n\t"
struct Sets { v8u a, b; };
#define SETS(p) "=&{v[132:139]}"(p.a), "=&{v[140:147]}"(p.b)
DEV void keep(const Sets& p) { asm volatile("" :: "{v[132:139]}"(p.a), "{v[140:147]}"(p.b)); }
DEV void o_proj(rsrc_t ws, const Quad (&q)[4], int cg, int head, int lane, const Tag& g) {
  asm volatile(ZERO4("100:101", "102:103") ZERO4("104:105", "106:107") "s_barrier\n\t"
    "ds_read_b128 v[108:111], %[aa]\n\tds_read_b128 v[112:115], %[aa] offset:256\n\tds_read_b128 v[116:119], %[aa] offset:512\n\tds_read_b128 v[120:123], %[aa] offset:768\n\t"
    LGKM(3) SM("100:103", "108:111", "x0") SM("104:107", "108:111", "y0") LGKM(2) SM("100:103", "112:115", "x1") SM("104:107", "112:115", "y1")
    LGKM(1) SM("100:103", "116:119", "x2") SM("104:107", "116:119", "y2") LGKM(0) SM("100:103", "120:123", "x3") SM("104:107", "120:123", "y3")
    "s_nop 7\n\t" FOLD(100, 101, 102, 103, 104, 105, 106, 107) PACK(100, 101, 102, 103)
    "buffer_store_dwordx4 v[100:103], %[vo], %[ws], %[so] offen sc1\n\ts_nop 1\n\t"
    :: [x0] "v"(q[0].x), [y0] "v"(q[0].y), [x1] "v"(q[1].x), [y1] "v"(q[1].y), [x2] "v"(q[2].x), [y2] "v"(q[2].y), [x3] "v"(q[3].x), [y3] "v"(q[3].y),
       [idx] "v"((lane & 3) * 0x5555u), [aa] "v"(L_A + (lane & 60) * 4), [vo] "v"(fold_off(lane)), [ws] "s"(ws), [so] "s"(WS_OP + cg * 16384 + head * 512), TAGS
    : "memory", V100);
}
DEV void h_pair(unsigned xv, int lane, unsigned& h0, unsigned& h1) {
  f32x2 p[8];
#pragma unroll
  for (int i = 0; i < 8; ++i) p[i] = *lp<f32x2>(L_A + lane * 8 + i * 512);
  const f32x2 t = ((p[0] + p[1]) + (p[2] + p[3])) + ((p[4] + p[5]) + (p[6] + p[7]));
  h0 = cvtpk(0.f, t[0] + bflo(xv)); h1 = cvtpk(0.f, t[1] + bfhi(xv));
}
DEV void final_sum(rsrc_t ws, u16* out, int ri, int k, int lane, const Tag& g) {
  const int l = lane & 31, bb = (ri >> 1) & 1;
  lds_wait_slow(C_HB, 1);
  const float y = *lp<float>(L_HB + alane(16 * (ri & 1) + (l >> 1), bb) * 8 + (l & 1) * 4);
  unsigned n, r; unsigned long long ea;
  asm volatile("s_mov_b32 %[n], %[sp]\n\ts_mov_b64 %[ea], -1\n\tv_mov_b32 v117, 1\n\tv_mov_b32 v118, %[cr]\n\t"
    "s_branch .Lgo%=\n"
    ".Lre%=:\n\ts_mov_b64 %[ea], vcc\n\ts_sub_u32 %[n], %[n], 1\n\ts_cbranch_scc1 .Lout%=\n.if " STR(FSL) "\n\ts_sleep " STR(FSL) "\n.endif\n"
    ".Lgo%=:\n\ts_mov_b64 exec, %[ea]\n\tbuffer_load_dwordx4 v[100:103], %[vd], %[ws], %[sa] offen sc1\n\ts_mov_b64 exec, -1\n\ts_waitcnt vmcnt(0)\n\t"
    UNTAG(104, 105, 106, 107, 100, 101, 102, 103) "v_or3_b32 v108, v104, v105, v106\n\tv_bitop3_b32 v108, v108, %[k], v107 bitop3:0xc8\n\tv_cmp_ne_u32 vcc, 0, v108\n\ts_cbranch_vccnz .Lre%=\n"
    ".Lout%=:\n\t"
    "v_permlane32_swap_b32 v104, v105\n\tv_permlane32_swap_b32 v106, v107\n\tv_add_f32 v104, v104, v105\n\tv_add_f32 v106, v106, v107\n\ts_nop 1\n\t"
    "v_permlane16_swap_b32 v104, v106\n\tv_add_f32 v104, v104, v106\n\ts_nop 1\n\tv_add_f32_dpp v104, v104, v104 row_ror:8" DPPM "0xf\n\t"
    "ds_write_b32 %[wa], v104\n\ts_mov_b64 exec, 1\n\tds_add_rtn_u32 v116, v118, v117\n\ts_mov_b64 exec, -1\n\t"
    "ds_read_b32 v108, %[ra] offset:1024\n\tds_read2_b32 v[100:101], %[ra] offset1:32\n\tds_read2_b32 v[102:103], %[ra] offset0:64 offset1:96\n\t"
    "ds_read2_b32 v[104:105], %[ra] offset0:128 offset1:160\n\tds_read2_b32 v[106:107], %[ra] offset0:192 offset1:224\n\t"
    LGKM(5) "v_readfirstlane_b32 %[r], v116\n\ts_cmp_lg_u32 %[r], 8\n\ts_cbranch_scc1 .Lskip%=\n\t"
    LGKM(2) "v_add_f32 v108, v108, %[y]\n\t" PKA("100:101", "100:101", "102:103") LGKM(0) PKA("104:105", "104:105", "106:107") "v_add_f32 v108, v108, v100\n\t"
    "v_add_f32 v104, v104, v101\n\tv_add_f32 v108, v108, v105\n\tv_add_f32 v108, v108, v104\n\tv_cvt_pk_bf16_f32 v108, v108, v108\n\t"
    "buffer_store_short v108, %[vo], %[ro], 0 offen\n"
    ".Lskip%=:\n\ts_waitcnt lgkmcnt(0)"
    : [n] "=&s"(n), [r] "=&s"(r), [ea] "=&s"(ea)
    : [vd] "v"((lane >> 3) * 4608 + (lane & 7) * 16), [ws] "s"(ws), [sa] "s"(WS_DP + 8 * k * 4608 + ri * 128), [k] "s"(0x7ffu), [sp] "n"(SPIN_G), [cr] "n"(C_R),
      [wa] "v"(L_RP + k * 128 + (4 * (lane & 7) + (lane >> 5) + 2 * ((lane >> 4) & 1)) * 4), [ra] "v"(L_RP + l * 4), [y] "v"(y),
      [vo] "v"(lo32(lane, (bb * D + (ri >> 2) * 64 + (ri & 1) * 32 + l) * 2)), [ro] "s"(mkrs(out, 2 * D * 2)), TAGS
    : "memory", "vcc", "scc", V100, V124);
}
DEV unsigned koff(int lane) { return ((lane >> 2) & 3) * 256 + (4 * (lane >> 4) + (lane & 3)) * 16; }
template <int N, int NW, int CP, int QS> DEV void attend(const u16* kc, const u16* vc, rsrc_t ws, rsrc_t rw, unsigned vw, unsigned vk, unsigned vv, unsigned so, int cu, int sw, int lane,
                                                         Quad (&q)[4], Tag& g, const v4u (&F)[3]) {
  const rsrc_t rk = mkrs(kc, 2u * 16 * SK * 256), rv = mkrs(vc, 2u * 16 * SK * 256);
  const unsigned idx = (lane & 3) * 0x5555u;
  constexpr float C2 = 0.08838834764831845f * 1.4426950408889634f;
  v4u W[16], K[N], V[N], qb; float sc[N], mx = -INFINITY;
  const unsigned t0 = now();
#pragma unroll
  for (int t = 0; t < 3; ++t) (NW ? W[t] : K[t]) = F[t];
#pragma unroll
  for (int t = 3; t < NW + N; ++t) {
    if (t == CP) { PIN(); asm volatile("s_barrier" ::: "memory"); PIN(); }
    if (t >= CP) room_at<CP, 0>(t, t0);
    if (t < NW) W[t] = ld16<AUX_NT>(rw, vw, (16 * (t >> 1) + (t & 1)) * (D * 2));
    else { K[t - NW] = ld16<AUX_NT>(rk, vk, so); so = so == (N - 1) * 1024 ? 0 : so + 1024; }
    if (NW && t >= CP + 3 && t < CP + 19 && ((t - CP - 3) & 3) == 0) { const int i = (t - CP - 3) >> 2; PIN(); q[i] = regroup(W[4 * i], W[4 * i + 1], W[4 * i + 2], W[4 * i + 3]); PIN(); }
  }
#pragma unroll
  for (int t = 0; t < N; ++t) {
    room_at<CP, 0>(NW + N + t, t0);
    V[t] = ld16<AUX_NT>(rv, vv, so); so = so == (N - 1) * 1024 ? 0 : so + 1024;
    PIN();
    if (t == QS) { lds_wait_slow(C_QF, 1); g = lds_tag(); qb = *lp<v4u>(L_Q + ((lane >> 3) & 1) * 256 + (4 * (lane >> 4) + (lane & 3)) * 16); }
    if (t >= QS) {
#pragma unroll
      for (int k = 2 * (t - QS); k < (t == N - 1 ? N : 2 * (t - QS) + 2) && k < N; ++k) {
        f32x4 d = mfma32(K[k], qb, f32x4{0, 0, 0, 0});
        sc[k] = (dpp<0x00>(d[0]) + dpp<0x55>(d[1]) + dpp<0xAA>(d[2]) + dpp<0xFF>(d[3])) * C2;
        mx = fmaxf(mx, sc[k]);
      }
    }
  }
  PIN();
  lds_inc(C_I);
  mx = sw32max(sw16max(mx));
  const int hs = (lane >> 3) & 1, ha = lane >> 5;
  const unsigned mA = (lane & 4) ? 0u : ~0u;
  v2u A[N]; float l = 0;
#pragma unroll
  for (int t = 0; t < N; ++t) { float p = __builtin_amdgcn_exp2f(sc[t] - mx); l += p; unsigned cc = hilo(p); A[t] = v2u{cc & mA, cc & ~mA}; }
  l = sw16add(l, l); l = sw32add(l, l);
  *lp<float>(L_MX + hs * 32 + sw * 4) = mx; *lp<float>(L_L1 + hs * 32 + sw * 4) = l;
  lds_inc(C_M);
  f32x4 acc = {0, 0, 0, 0};
#pragma unroll
  for (int t = 0; t < N - CP; ++t) acc = sm32(A[t], V[t], acc, idx);
  PIN();
  lds_wait(C_M, 8);
  const float mi = *lp<float>(L_MX + ha * 32 + (lane & 7) * 4);
  float M = fmaxf(mi, dpp<0xB1>(mi)); M = fmaxf(M, dpp<0x4E>(M)); M = fmaxf(M, dpp<0x141>(M));
  float f = __builtin_amdgcn_exp2f(*lp<float>(L_MX + ha * 32 + sw * 4) - M);
  { float L = *lp<float>(L_L1 + ha * 32 + (lane & 7) * 4) * __builtin_amdgcn_exp2f(mi - M);
    L += dpp<0xB1>(L); L += dpp<0x4E>(L); L += dpp<0x141>(L);
    publish(ws, ((lane & 31) | sw) ? 0x80000000u : 1024 + ha * 16, WS_AP + cu * 1152, pack(f32x4{M, asf(asu(M) << 21), L, L}, g)); }
  unsigned wa = L_P1 + sw * 1024 + lane * 16, ra = L_P1 + lane * 16, cf = C_F, one = 1, vo = (ha * 32 + 2 * (lane & 15) + ((lane >> 4) & 1)) * 16;
  asm volatile("" : "+v"(f), "+v"(wa), "+v"(ra), "+v"(cf), "+v"(one), "+v"(vo)); PIN();
#pragma unroll
  for (int t = N - CP; t < N; ++t) acc = sm32(A[t], V[t], acc, idx);
  acc *= f;
  unsigned r;
  asm volatile("ds_write_b128 %[wa], %[p]\n\ts_mov_b64 exec, 1\n\tds_add_rtn_u32 v132, %[cf], %[one]\n\ts_mov_b64 exec, -1\n\t"
    "ds_read_b128 v[100:103], %[ra]\n\tds_read_b128 v[104:107], %[ra] offset:1024\n\tds_read_b128 v[108:111], %[ra] offset:2048\n\tds_read_b128 v[112:115], %[ra] offset:3072\n\t"
    "ds_read_b128 v[116:119], %[ra] offset:4096\n\tds_read_b128 v[120:123], %[ra] offset:5120\n\tds_read_b128 v[124:127], %[ra] offset:6144\n\tds_read_b128 v[128:131], %[ra] offset:7168\n\t"
    LGKM(8) "v_readfirstlane_b32 %[r], v132\n\ts_cmp_lg_u32 %[r], 7\n\ts_cbranch_scc1 .Lskip%=\n\t"
    LGKM(6) PKA("100:101", "100:101", "104:105") PKA("102:103", "102:103", "106:107") LGKM(5) PKA("100:101", "100:101", "108:109") PKA("102:103", "102:103", "110:111")
    LGKM(4) PKA("100:101", "100:101", "112:113") PKA("102:103", "102:103", "114:115") LGKM(3) PKA("100:101", "100:101", "116:117") PKA("102:103", "102:103", "118:119")
    LGKM(2) PKA("100:101", "100:101", "120:121") PKA("102:103", "102:103", "122:123") LGKM(1) PKA("100:101", "100:101", "124:125") PKA("102:103", "102:103", "126:127")
    LGKM(0) PKA("100:101", "100:101", "128:129") PKA("102:103", "102:103", "130:131") PACK(100, 101, 102, 103)
    "buffer_store_dwordx4 v[100:103], %[vo], %[ws], %[so] offen sc1\n\ts_mov_b64 exec, 1\n\tds_add_u32 %[cf], %[one]\n\ts_mov_b64 exec, -1\n"
    ".Lskip%=:\n\ts_waitcnt lgkmcnt(0)"
    : [r] "=&s"(r)
    : [p] "v"(acc), [wa] "v"(wa), [ra] "v"(ra), [cf] "v"(cf), [one] "v"(one), [vo] "v"(vo), [ws] "s"(ws), [so] "s"(WS_AP + cu * 1152), TAGS
    : "memory", "scc", V100, V8(124, 125, 126, 127, 128, 129, 130, 131), "v132");
}

#define ROLES() \
  int c = blockIdx.x, tid = threadIdx.x; asm volatile("" : "+s"(c)); asm volatile("" : "+v"(tid)); \
  const int lane = tid & 63, xg = c & 7, ic = c >> 3, kvh = 2 * xg + (ic >> 4), j = ic & 15, b = j >> 3, s = j & 7, cu = kvh * 16 + j; \
  const int n = ic - 1 - (ic >> 3), head = 2 * kvh + b, rg = n >> 1, mat = n & 1, di = 9 * xg + n - 18, ri = 4 * rg + (xg >> 1); \
  const bool isO = s == 0, isG = !isO && n < 18, isD = !isO && !isG && n < 27, isR = isG && !mat && !((xg ^ rg) & 1); \
  const unsigned idx = (lane & 3) * 0x5555u; \
  const int ab = (lane >> 3) & 1, ar = 8 * (lane >> 4) + (lane & 7);     \
  const rsrc_t ws = mkrs(wsp, WS_END), rx = mkrs(x, 2 * D * 2), ro = mkrs(wo, 4096 * D * 2); (void)idx; (void)isD; (void)isR; (void)ri; (void)di; (void)ab; (void)ar; (void)rx; (void)ro; (void)cu; (void)head
#define FIN() __builtin_amdgcn_endpgm()
template <typename T> DEV T* argp(unsigned lo, unsigned hi) { return (T*)(((unsigned long long)hi << 32) | lo); }
__global__ void __launch_bounds__(768, 3)
fused_decode_kernel(const u16* x_, const u16* kc_, const u16* vc_, const u16* wq_, const u16* wo_, const u16* w1_, const u16* w3_, const u16* w2_, u16* out_, unsigned* ws_) {
  __shared__ v4u lds_all[L_ALL / 16];
  v16u ka; v4u kb; const u16* pf; int w; unsigned long long tz;
  unsigned v0, s0, s1, s2, sk, key0;
  { unsigned kvh, t0, t1, t2, sw, va, vb;
    asm volatile(
      "s_load_dwordx16 %[ka], %[arg], 0x0\n\ts_load_dwordx4 %[kb], %[arg], 0x40\n\t"
      "v_readfirstlane_b32 %[w], %[tid]\n\ts_and_b32 %[t0], %[c], 7\n\ts_lshr_b32 %[t1], %[c], 7\n\ts_lshl1_add_u32 %[kvh], %[t0], %[t1]\n\t"
      "s_lshr_b32 %[w], %[w], 6\n\tv_lshlrev_b32 %[va], 4, %[tid]\n\ts_sub_u32 %[sw], %[w], 4\n\ts_cbranch_scc1 .Laux%=\n\t"
      "s_bfe_u32 %[t2], %[c], 0x10006\n\ts_mul_i32 %[t0], %[c], %[rc]\n\ts_mul_i32 %[t1], %[sw], %[rw]\n\ts_add_u32 %[sk], %[t0], %[t1]\n\t"
      "s_lshl4_add_u32 %[key], %[t2], %[kvh]\n\ts_lshl_b32 %[key], %[key], 15\n\ts_bfe_u32 %[t0], %[c], 0x30003\n\ts_cbranch_scc0 .Lo%=\n\t"
      "s_load_dwordx2 %[pf], %[arg], 0x8\n\ts_lshl3_add_u32 %[t0], %[t0], %[sw]\n\ts_mul_i32 %[t0], %[t0], 68\n\ts_add_u32 %[key], %[key], %[t0]\n\t"
      "s_lshl_b32 %[key], %[key], 8\n\ts_add_u32 %[key], %[key], 0x6f0000\n\t"
      "s_mul_hi_u32 %[t0], %[sk], 0xf0f0f0f1\n\ts_lshr_b32 %[t0], %[t0], 4\n\ts_mul_i32 %[t0], %[t0], 17\n\ts_sub_u32 %[t0], %[sk], %[t0]\n\ts_lshl_b32 %[s0], %[t0], 10\n\t"
      "v_lshlrev_b32 %[vb], 6, %[tid]\n\tv_and_b32 %[va], 0x30, %[va]\n\tv_and_b32 %[vb], 0x300, %[vb]\n\tv_lshlrev_b32 %[v0], 2, %[tid]\n\tv_and_b32 %[v0], 0xc0, %[v0]\n\t"
      "v_or3_b32 %[v0], %[v0], %[va], %[vb]\n\tv_add_u32 %[v0], %[key], %[v0]\n\t"
      "s_add_u32 %[s1], %[s0], 0x400\n\ts_cmpk_lg_i32 %[s0], 0x4000\n\ts_cselect_b32 %[s1], %[s1], 0\n\t"
      "s_add_u32 %[s2], %[s1], 0x400\n\ts_cmpk_lg_i32 %[s1], 0x4000\n\ts_cselect_b32 %[s2], %[s2], 0\n\t"
      "s_add_u32 %[sk], %[s2], 0x400\n\ts_cmpk_lg_i32 %[s2], 0x4000\n\ts_cselect_b32 %[sk], %[sk], 0\n\ts_branch .Ljoin%=\n"
      ".Lo%=:\n\ts_load_dwordx2 %[pf], %[arg], 0x20\n\ts_mul_i32 %[t0], %[sw], 36\n\ts_add_u32 %[key], %[key], %[t0]\n\ts_lshl_b32 %[key], %[key], 8\n\ts_add_u32 %[key], %[key], 0x700000\n\t"
      "s_mul_hi_u32 %[t0], %[sk], 0x38e38e39\n\ts_lshr_b32 %[t0], %[t0], 1\n\ts_mul_i32 %[t0], %[t0], 9\n\ts_sub_u32 %[t0], %[sk], %[t0]\n\ts_lshl_b32 %[sk], %[t0], 10\n\t"
      "s_lshl1_add_u32 %[t0], %[kvh], %[t2]\n\ts_mul_i32 %[t0], %[t0], 0x24000\n\ts_lshl_b32 %[t1], %[sw], 7\n\ts_add_u32 %[t0], %[t0], %[t1]\n\t"
      "v_lshrrev_b32 %[vb], 2, %[tid]\n\tv_lshrrev_b32 %[v0], 3, %[tid]\n\tv_and_b32 %[vb], 8, %[vb]\n\tv_and_or_b32 %[vb], %[v0], 2, %[vb]\n\tv_lshrrev_b32 %[v0], 1, %[tid]\n\t"
      "v_and_or_b32 %[vb], %[v0], 4, %[vb]\n\tv_and_b32 %[va], 0x70, %[va]\n\tv_mul_u32_u24 %[vb], 0x480, %[vb]\n\tv_add3_u32 %[v0], %[vb], %[va], %[t0]\n\t"
      "s_mov_b32 %[s0], 0\n\ts_movk_i32 %[s1], 0x480\n\ts_movk_i32 %[s2], 0x4800\n\ts_branch .Ljoin%=\n"
      ".Laux%=:\n\ts_load_dwordx2 %[pf], %[arg], 0x18\n\ts_memrealtime %[tz]\n\ts_setprio 3\n\ts_bfe_u32 %[t0], %[c], 0x20005\n\ts_lshl2_add_u32 %[t0], %[t0], %[w]\n\ts_mul_i32 %[t0], %[t0], 0x48000\n\t"
      "s_bfe_u32 %[t1], %[c], 0x20003\n\ts_lshl2_add_u32 %[t1], %[kvh], %[t1]\n\ts_lshl_b32 %[t1], %[t1], 7\n\ts_add_u32 %[t0], %[t0], %[t1]\n\t"
      "v_lshlrev_b32 %[vb], 2, %[tid]\n\tv_mov_b32 %[v0], 0\n\tv_and_b32 %[vb], 60, %[vb]\n\tds_write_b32 %[vb], %[v0]\n\t"
      "v_lshlrev_b32 %[vb], 12, %[tid]\n\tv_and_b32 %[va], 0x70, %[va]\n\tv_and_b32 %[vb], 0x38000, %[vb]\n\tv_add3_u32 %[v0], %[vb], %[va], %[t0]\n\t"
      "s_mov_b32 %[s0], 0\n\ts_movk_i32 %[s1], 0x2000\n\ts_movk_i32 %[s2], 0x4000\n"
      ".Ljoin%=:\n\ts_waitcnt lgkmcnt(0)"
      : [ka] "=&s"(ka), [kb] "=&s"(kb), [pf] "=&s"(pf), [w] "=&s"(w), [v0] "=&v"(v0), [s0] "=&s"(s0), [s1] "=&s"(s1), [s2] "=&s"(s2), [sk] "=&s"(sk), [key] "=&s"(key0), [tz] "=&s"(tz),
        [kvh] "=&s"(kvh), [t0] "=&s"(t0), [t1] "=&s"(t1), [t2] "=&s"(t2), [sw] "=&s"(sw), [va] "=&v"(va), [vb] "=&v"(vb)
      : [arg] "s"(__builtin_amdgcn_kernarg_segment_ptr()), [tid] "v"(threadIdx.x), [c] "s"(blockIdx.x), [rc] "n"(ROT_C), [rw] "n"(ROT_W), "s"((LDS v4u*)lds_all)
      : "memory", "scc"); }
  static_assert(((SK - WIN) + 32 * NKO - 8 * 4 * NK) * 256 == 0x6f0000 && (SK - WIN) * 256 == 0x700000 && NK == 17 && NKO == 9, "constants above");
  v4u F[3];
  { const rsrc_t rf = mkrs(pf, 2u * 16 * SK * 256);
    F[0] = ld16<AUX_NT>(rf, v0, s0); F[1] = ld16<AUX_NT>(rf, v0, s1); F[2] = ld16<AUX_NT>(rf, v0, s2); PIN(); }
  const u16 *x = argp<const u16>(ka[0], ka[1]), *kc = argp<const u16>(ka[2], ka[3]), *vc = argp<const u16>(ka[4], ka[5]), *wq = argp<const u16>(ka[6], ka[7]),
            *wo = argp<const u16>(ka[8], ka[9]), *w1 = argp<const u16>(ka[10], ka[11]), *w3 = argp<const u16>(ka[12], ka[13]), *w2 = argp<const u16>(ka[14], ka[15]);
  u16* out = argp<u16>(kb[0], kb[1]); unsigned* wsp = argp<unsigned>(kb[2], kb[3]);
  Tag g;
  if (w >= 4) {
    Quad q[4]; const int sw = w - 4;
    if ((blockIdx.x & 56) == 0) {
      ROLES();
      attend<NKO, 16, CAPO, QAO>(kc, vc, ws, ro, v0, key0 + koff(lane), key0 + lane * 16, sk, cu, sw, lane, q, g, F);
      o_proj(ws, q, sw, head, lane, g);
      FIN();
    } else {
      ROLES();
      attend<NK, 0, CAP, QA>(kc, vc, ws, ro, 0, v0, key0 + lane * 16, sk, cu, sw, lane, q, g, F);
      if (isG) {
        const unsigned ha = L_A + sw * 512 + alane(2 * (lane & 15) + (lane >> 5), (lane >> 4) & 1) * 8;
        unsigned xv = 0;
        if (sw == 1) xv = __builtin_amdgcn_raw_buffer_load_b32(rx, (ab * D + 64 * rg + 2 * ar) * 2, 0, 0);
        lds_wait_slow(C_WI, 4);
        Sets ps;
        { unsigned n; unsigned long long ea, eb;
          asm volatile("s_mov_b32 %[n], %[sp]\n\ts_mov_b64 %[ea], -1\n\ts_mov_b64 %[eb], -1\n\t"
            "s_branch .Lgo%=\n" RETRY2("s_sleep %[sl]\n")

            PKA("108:109", "108:109", "112:113") PKA("110:111", "110:111", "114:115") "s_nop 1\n\tv_permlane32_swap_b32 v108, v110\n\tv_permlane32_swap_b32 v109, v111\n\t"
            PKA("108:109", "108:109", "110:111") "ds_write_b64 %[wa], v[108:109]\n\t" LGKM(0) "s_barrier"
            : [n] "=&s"(n), [ea] "=&s"(ea), [eb] "=&s"(eb), SETS(ps)
            : [vd] "v"(lane * 16), [ws] "s"(ws), [sa] "s"(WS_OP + rg * 16384 + sw * 2048), [sb] "s"(WS_OP + rg * 16384 + sw * 2048 + 1024), [k] "s"(0x7ffu), [sp] "n"(SPIN_G), [sl] "n"(HSL),
              [wa] "v"(ha), TAGS
            : "memory", "vcc", "scc", V100); }
        if (sw == 2 && (rg | mat | xg) == 0) publish(ws, lane ? 0x80000000u : 0u, 0, v4u{g.t[0], g.t[1], g.t[2], g.t[3]});
        if (sw == 1 && !mat) {
          unsigned h0, h1; h_pair(xv, lane, h0, h1);
          float t = sw32add(asf(h0) + asf(h1), asf(h0) * asf(h0) + asf(h1) * asf(h1));
          t = sw16add(t, t); t += dpp<0xB1>(t); t += dpp<0x4E>(t); t += dpp<0x141>(t);
          const float hi = asf(asu(t) & g.m);
          publish(ws, (lane & 23) ? 0x80000000u : ((lane >> 5) * 2 + ab) * 16, WS_ST + (xg * 9 + rg) * 128, pack(f32x4{hi, t - hi, hi, hi}, g));
          *lp<v2u>(L_HB + lane * 8) = v2u{h0, h1};
          lds_inc(C_HB);
        }
        keep(ps);
        if (isR) final_sum(ws, out, ri, sw, lane, g);
        FIN();
      } else if (isD) {
        if (PT && sw == 7) touch(ws, WS_GU + di * 9216 + lane * 128, sel(lane < 8, WS_GU + di * 9216 + 8192 + lane * 128, sel(lane < 44, WS_CS + di * 4608 + (lane - 8) * 128, WS_ST + (xg * 9 + min(lane - 44, 8)) * 128)), 0);
        if (sw == 6) {
          lds_wait_slow(C_WI, 4);
          f32x4 u[5];
          sweep<5, CSSL>(ws, u, g, [&](int i, unsigned& v, unsigned& so) { v = (i < 4 ? lane : lane & 31) * 16; so = WS_CS + di * 4608 + i * 1024; });
          const f32x4 z = {0, 0, 0, 0}, a = (u[0] + u[1]) + (u[2] + u[3]) + (lane < 32 ? u[4] : z);
          *lp<f32x2>(L_CS + (lane & 31) * 16 + (lane >> 5) * 8) = f32x2{sw32add(a[0], a[2]), sw32add(a[1], a[3])};
          lds_inc(C_CS);
          bar();
        } else if (sw == 5) {
          lds_wait_slow(C_CS, 1);
          f32x4 u[1];
          sweep<1, 0>(ws, u, g, [&](int i, unsigned& v, unsigned& so) { v = ((lane & 15) < 9 ? (lane & 15) * 128 : 0) + (lane >> 4) * 16; so = WS_ST + xg * 9 * 128; });
          float t = (lane & 15) < 9 ? u[0][0] + u[0][1] : 0.f;
          t += dpp<0xB1>(t); t += dpp<0x4E>(t); t += dpp<0x124>(t); t += dpp<0x128>(t);
          const auto sp = __builtin_amdgcn_permlane32_swap(asu(t), asu(t), false, false);
          const float m = asf(sp[0]) * (1.f / D), var = fmaxf(asf(sp[1]) * (1.f / D) - m * m, 0.f);
          *lp<f32x2>(L_ST + ((lane >> 4) & 1) * 8) = f32x2{m, __builtin_amdgcn_rsqf(var + 1e-5f)};
          bar();
        } else if (sw < 5) {
          const unsigned ga = L_A + sw * 1024 + (lane >> 4) * 256 + ((lane * 16 + (lane & 16) * 8) & 255);
          lds_wait_slow(C_CS, 1);
          unsigned n; unsigned long long ea, eb = (unsigned long long)(long long)__builtin_amdgcn_readfirstlane((sw - 4) >> 31);
          asm volatile("s_mov_b32 %[n], %[sp]\n\ts_mov_b64 %[ea], -1\n\t"
            "v_mov_b32 v104, %[t0]\n\tv_mov_b32 v105, %[t1]\n\tv_mov_b32 v106, %[t2]\n\tv_mov_b32 v107, %[t3]\n\ts_branch .Lgo%=\n" RETRY2(".if " STR(DSL) "\n\ts_sleep " STR(DSL) "\n.endif\n")

            PKA("108:109", "108:109", "112:113") PKA("110:111", "110:111", "114:115") "ds_write_b128 %[wa], v[108:111]\n\t" LGKM(0) "s_barrier"
            : [n] "=&s"(n), [ea] "=&s"(ea), [eb] "+s"(eb)
            : [vd] "v"(lane * 16), [ws] "s"(ws), [sa] "s"(WS_GU + di * 9216 + sw * 2048), [sb] "s"(WS_GU + di * 9216 + sw * 2048 + 1024), [k] "s"(0x7ffu), [sp] "n"(SPIN_G), [wa] "v"(ga), TAGS
            : "memory", "vcc", "scc", V100, V124);
        } else bar();
      }
      FIN();
    }
  } else {
    v4u wv[5], ep, xs0, xs1; v2u xa[2]; unsigned xs2, xt[2];
    { const int lane = threadIdx.x & 63, r0 = 144 * ((blockIdx.x >> 5) & 3) + 36 * w, gp = lane >> 3, ap = 2 * (lane >> 4) + ((lane >> 3) & 1);
      const rsrc_t rq = mkrs(wq, D * 4096 * 2), rx = mkrs(x, 2 * D * 2);
      wv[0] = F[0]; wv[1] = F[1]; wv[2] = F[2]; wv[3] = ld16<AUX_NT>(rq, v0, 3 * 8192);
      wv[4] = ld16<AUX_NT>(rq, (v0 + (32 - 3 * gp) * 8192) | (gp < 4 ? 0 : 0x80000000u), 0);
      ep = ld16<AUX_SC1>(mkrs(wsp, 16), 0, 0);
      const unsigned xo = (lane >> 5) * (D * 2);
      xs0 = ld16<0>(rx, xo + (lane & 31) * 16, 0); xs1 = ld16<0>(rx, xo + (lane & 31) * 16, 512);
      xs2 = __builtin_amdgcn_raw_buffer_load_b32(rx, xo + (lane & 31) * 4, 1024, 0);
#pragma unroll
      for (int bb = 0; bb < 2; ++bb) { xa[bb] = __builtin_amdgcn_raw_buffer_load_b64(rx, (r0 + 4 * ap) * 2, bb * D * 2, 0);
        xt[bb] = __builtin_amdgcn_raw_buffer_load_b16(rx, (r0 + 32 + (ap & 3)) * 2, bb * D * 2, 0); }
      PIN(); }
    ROLES();
    auto prewrite = [&]() { const v4u z = {0, 0, 0, 0};
      { if (w == 2) { publish(ws, lane * 16, WS_AP + cu * 1152, z); publish(ws, lane < 8 ? 1024 + lane * 16 : 0x80000000u, WS_AP + cu * 1152, z); } }
    };
    {
      prewrite();
      PIN(); asm volatile("s_barrier" ::: "memory"); PIN();
      const unsigned xs[9] = {xs0[0], xs0[1], xs0[2], xs0[3], xs1[0], xs1[1], xs1[2], xs1[3], xs2};
      float sm = 0;
#pragma unroll
      for (int k = 0; k < 9; ++k) sm += bflo(xs[k]) + bfhi(xs[k]);
      const float m = sum32(sm) * (1.f / D); float q = 0;
#pragma unroll
      for (int k = 0; k < 9; ++k) { float d0 = bflo(xs[k]) - m, d1 = bfhi(xs[k]) - m; q += d0 * d0 + d1 * d1; }
      const float r = __builtin_amdgcn_rsqf(sum32(q) * (1.f / D) + 1e-5f);
      const unsigned mA = (lane & 4) ? 0u : ~0u;
      f32x4 a[2];
#pragma unroll
      for (int bb = 0; bb < 2; ++bb) {
        const float mm = asf(__builtin_amdgcn_readlane(asu(m), 32 * bb)), rr = asf(__builtin_amdgcn_readlane(asu(r), 32 * bb));
        const float xv[5] = {bflo(xa[bb][0]), bfhi(xa[bb][0]), bflo(xa[bb][1]), bfhi(xa[bb][1]), bflo(xt[bb])};
        a[bb] = f32x4{0, 0, 0, 0};
#pragma unroll
        for (int t = 0; t < 5; ++t) { unsigned cc = hilo((xv[t] - mm) * rr); a[bb] = sm32(v2u{cc & mA, cc & ~mA}, wv[t], a[bb], idx); }
      }
      f32x4 qp;
#pragma unroll
      for (int k = 0; k < 4; ++k) { auto sp = __builtin_amdgcn_permlane32_swap(asu(a[0][k]), asu(a[1][k]), false, false); qp[k] = asf(sp[0]) + dpp<0x128>(asf(sp[1])); }
      *lp<f32x4>(sel(lane & 8, L_P1 + lane * 16, L_QP + w * 512 + fold_off(lane))) = qp;
      vmw<0>();
      if (lds_inc_rtn(C_Q) == 3) {
#pragma unroll
        for (int i = 0; i < 4; ++i) g.t[i] = ((__builtin_amdgcn_readfirstlane(ep[i]) + 1) & 0x3ffu) | 0x400u;
        g.m = vmask();
        const f32x4 y = *lp<f32x4>(L_QP + lane * 16) + *lp<f32x4>(L_QP + 1024 + lane * 16);
        const f32x4 yy = {sw32add(y[0], y[0]), sw32add(y[1], y[1]), sw32add(y[2], y[2]), sw32add(y[3], y[3])};
        publish(ws, lo32(lane, (lane >> 4) * 4096 + (lane & 15) * 16), WS_QP + kvh * 8192 + (j >> 2) * 1024 + (j & 3) * 256, pack(yy, g));
        __builtin_amdgcn_s_sleep(16);
        f32x4 u[4];
        sweep<4, 8>(ws, u, g, [&](int i, unsigned& v, unsigned& so) { v = lane * 16; so = WS_QP + kvh * 8192 + b * 4096 + i * 1024; });
        const f32x4 qq = (u[0] + u[1]) + (u[2] + u[3]);
        *lp<v2u>(L_Q + lane * 8) = v2u{cvtpk(qq[0], qq[1]), cvtpk(qq[2], qq[3])};
        *lp<v4u>(L_TAG) = v4u{g.t[0], g.t[1], g.t[2], g.t[3]};
        lds_inc(C_QF);
      }
    }
    __builtin_amdgcn_s_setprio(PRIO);
    if (isO) {
      v4u W[4]; const unsigned vw = (head * 128 + 32 * w + 16 * ab + 8 * ((lane >> 4) & 1) + 2 * (lane >> 5)) * (D * 2) + 1024 + (lane & 7) * 16;
#pragma unroll
      for (int t = 0; t < 4; ++t) { if (t >= CAPA) vmw<CAPA - 1>(); W[t] = ld16<AUX_NT>(ro, vw, (4 * (t & 1) + (t >> 1)) * (D * 2)); gap(); }
      const Quad q = regroup(W[0], W[1], W[2], W[3]);
      const unsigned pa = (ab * 8 + (lane >> 4)) * 1152, a0 = WS_AP + kvh * 16 * 1152;
      lds_wait_slow(C_F, 8); g = lds_tag(); lds_wait(C_F, 9);
      unsigned n, r; unsigned long long ea, eb, em;
      asm volatile("s_mov_b32 %[n], %[sp]\n\t" ZERO4("124:125", "126:127") ZERO4("128:129", "130:131") "v_mov_b32 v122, 1\n\tv_mov_b32 v123, %[ca]\n\t"
        ".Lml%=:\n\tbuffer_load_dwordx4 v[118:121], %[vm], %[ws], %[sa] offen sc1\n\tbuffer_load_dwordx4 v[100:103], %[vd], %[ws], %[sa] offen sc1\n\t"
        "buffer_load_dwordx4 v[104:107], %[vd], %[ws], %[sb] offen sc1\n\ts_sub_u32 %[n], %[n], 1\n\ts_waitcnt vmcnt(2)\n\t" UNTAG(108, 109, 110, 111, 118, 119, 120, 121)
        "v_or3_b32 v116, v108, v109, v110\n\tv_bitop3_b32 v116, v116, %[k], v111 bitop3:0xc8\n\tv_cmp_ne_u32 vcc, 0, v116\n\ts_cbranch_scc1 .Lf%=\n\ts_cbranch_vccnz .Lml%=\n"
        ".Lf%=:\n\t" "v_lshrrev_b32 v109, 21, v109\n\tv_or_b32 v108, v108, v109\n\ts_nop 1\n\tv_max_f32_dpp v111, v108, v108 quad_perm:[1,0,3,2]" DPPM "0xf\n\t"
        "v_mov_b32 v112, v111\n\ts_nop 1\n\tv_permlane16_swap_b32 v111, v112\n\tv_max_f32 v111, v111, v112\n\tv_mov_b32 v112, v111\n\ts_nop 1\n\tv_permlane32_swap_b32 v111, v112\n\tv_max_f32 v111, v111, v112\n\t"
        "v_sub_f32 v108, v108, v111\n\tv_exp_f32 v108, v108\n\ts_nop 0\n\tv_mul_f32 v110, v108, v110\n\ts_nop 1\n\tv_add_f32_dpp v110, v110, v110 quad_perm:[1,0,3,2]" DPPM "0xf\n\t"
        "v_mov_b32 v112, v110\n\ts_nop 1\n\tv_permlane16_swap_b32 v110, v112\n\tv_add_f32 v110, v110, v112\n\tv_mov_b32 v112, v110\n\ts_nop 1\n\tv_permlane32_swap_b32 v110, v112\n\tv_add_f32 v110, v110, v112\n\t"
        "v_rcp_f32 v110, v110\n\ts_nop 0\n\tv_mul_f32 v108, v108, v110\n\ts_nop 1\n\tv_mov_b32_dpp v118, v108" QP(0) "0xf\n\tv_mov_b32_dpp v120, v108" QP(1) "0xf\n\ts_mov_b32 %[n], %[sp]\n\ts_branch .Lchk%=\n"
        RETRY2("\n")
        "v_pk_mul_f32 v[108:109], v[118:119], v[108:109] op_sel_hi:[0,1]\n\tv_pk_mul_f32 v[110:111], v[118:119], v[110:111] op_sel_hi:[0,1]\n\t"
        "v_pk_fma_f32 v[108:109], v[120:121], v[112:113], v[108:109] op_sel_hi:[0,1,1]\n\tv_pk_fma_f32 v[110:111], v[120:121], v[114:115], v[110:111] op_sel_hi:[0,1,1]\n\ts_nop 0\n\t"
        "v_permlane32_swap_b32 v108, v109\n\tv_permlane32_swap_b32 v110, v111\n\tv_add_f32 v108, v108, v109\n\tv_add_f32 v110, v110, v111\n\ts_nop 1\n\t"
        "v_permlane16_swap_b32 v108, v110\n\tv_add_f32 v108, v108, v110\n\tv_and_b32 v109, 0xffff0000, v108\n\tv_sub_f32 v108, v108, v109\n\tv_cvt_pk_bf16_f32 v108, v109, v108\n\t"
        "ds_write_b32 %[aa], v108\n\ts_nop 0\n\t" QUAD(112, 113, 114, 115, 108) LGKM(0) "s_barrier\n\t"
        SM("124:127", "112:115", "x") SM("128:131", "112:115", "y") "s_nop 7\n\t" FOLD(124, 125, 126, 127, 128, 129, 130, 131)
        "ds_write_b128 %[wa], v[124:127]\n\ts_mov_b64 exec, 1\n\tds_add_rtn_u32 v116, v123, v122\n\ts_mov_b64 exec, -1\n\t"
        "ds_read_b128 v[100:103], %[ra]\n\tds_read_b128 v[104:107], %[ra] offset:1024\n\tds_read_b128 v[108:111], %[ra] offset:2048\n\tds_read_b128 v[112:115], %[ra] offset:3072\n\t"
        LGKM(4) "v_readfirstlane_b32 %[r], v116\n\ts_cmp_lg_u32 %[r], 3\n\ts_cbranch_scc1 .Lskip%=\n\t"
        LGKM(2) PKA("100:101", "100:101", "104:105") PKA("102:103", "102:103", "106:107") LGKM(1) PKA("100:101", "100:101", "108:109") PKA("102:103", "102:103", "110:111")
        LGKM(0) PKA("100:101", "100:101", "112:113") PKA("102:103", "102:103", "114:115") PACK(100, 101, 102, 103)
        "buffer_store_dwordx4 v[100:103], %[vo], %[ws], %[so] offen sc1\n\ts_nop 0\n"
        ".Lskip%=:\n\ts_waitcnt lgkmcnt(0)"
        : [n] "=&s"(n), [r] "=&s"(r), [ea] "=&s"(ea), [eb] "=&s"(eb), [em] "=&s"(em)
        : [x] "v"(q.x), [y] "v"(q.y), [idx] "v"(idx), [vd] "v"(pa + b * 512 + w * 128 + (lane & 7) * 16), [vm] "v"(pa + (lane & 1) * 4 * 1152 + 1024 + b * 16),
          [aa] "v"(L_A + w * 256 + alane(4 * (lane & 7) + (lane >> 5) + 2 * ((lane >> 4) & 1), ab) * 4), [wa] "v"(L_P1 + w * 1024 + lane * 16), [ra] "v"(L_P1 + lane * 16), [vo] "v"(fold_off(lane)),
          [ws] "s"(ws), [sa] "s"(a0), [sb] "s"(a0 + 4 * 1152), [so] "s"(WS_OP + 8 * 16384 + head * 512), [k] "s"(0x7ffu), [sp] "n"(SPIN_G), [ca] "n"(C_A), TAGS
        : "memory", "vcc", "scc", V100, V124, V8(148, 149, 150, 151, 152, 153, 154, 155), V8(156, 157, 158, 159, 160, 161, 162, 163));
      FIN();
    } else if (isG || isD) {
      const unsigned rs = isG ? FF * 2 : D * 2;
      const rsrc_t rw = mkrs(isG ? (mat ? w3 : w1) : w2, FF * D * 2);
      const unsigned vb = isG ? 64 * rg * (FF * 2) + xg * (D * 2) : di * 64 * (D * 2);
      const unsigned vw = vb + (16 * (lane >> 5) + ((lane >> 4) & 1)) * rs + (lane & 15) * 16 + w * 256;
      const unsigned v3 = (vb + 2 * wrow(lane) * rs + (lane & 7) * 16 + 1024) | sel(w == 3, 0, 0x80000000u);
      unsigned xv = 0;
      if (isG) xv = __builtin_amdgcn_raw_buffer_load_b32(rx, (ab * D + 64 * rg + 2 * ar) * 2, 0, 0);
      lds_wait_slow(C_QF, 1);
      g = lds_tag();
      const unsigned vo = fold_off(lane), v2 = fold_off(lane & ~8), ps = isG ? 9216 : 512;
      const unsigned po = isG ? WS_GU + ((xg * 9 + 2 * w) * 9 + rg) * 1024 + mat * 512 : WS_DP + di * 4608 + w * 1024;
      unsigned st, n, te; static_assert(C_WI == 28 && C_CS == 24 && C_F == 16, "below");
#define WLD(d, v, r) "s_mul_i32 %[st], %[rs], " #r "\n\tbuffer_load_dwordx4 v[" d "], %[" v "], %[rw], %[st] offen nt\n\ts_nop 6\n\t"
#define SMF(d, a, b) "v_smfmac_f32_16x16x64_bf16 v[" d "], v[" a "], v[" b "], %[idx]\n\t"
#define CSUM(n, b) "s_waitcnt vmcnt(" #n ")\n\t" SMF("152:155", "148:151", b)
#define SWAP2(a, b, c, d) "v_swap_b32 v" #a ", v" #c "\n\tv_swap_b32 v" #b ", v" #d "\n\t"
#define WHEN(i) "s_add_u32 %[n], %[n], %[dt]\n.Lu" #i "_%=:\n\ts_min_u32 %[st], %[n], %[te]\n\ts_cmp_ge_u32 vcc_lo, %[st]\n\ts_cbranch_scc1 .Lt" #i "_%=\n\t" \
        "ds_read_b32 v157, v156\n\ts_memrealtime vcc\n\t" LGKM(0) "v_readfirstlane_b32 %[st], v157\n\ts_sub_u32 vcc_lo, vcc_lo, %[tz]\n\ts_cmp_ge_u32 %[st], " STR(BURSTN) "\n\ts_cselect_b32 %[te], %[tb], %[te]\n\t" \
        "s_branch .Lu" #i "_%=\n.Lt" #i "_%=:\n\t"
#define ISSUED "v_mov_b32 v156, 28\n\tv_mov_b32 v157, 1\n\ts_mov_b64 exec, 1\n\tds_add_u32 v156, v157\n\ts_mov_b64 exec, -1\n\t"
#define LAST64 SWAP2(74, 75, 80, 81) SWAP2(78, 79, 84, 85) SWAP2(90, 91, 96, 97) SWAP2(94, 95, 100, 101) \
        SMF("124:127", "144:147", "72:79") SMF("128:131", "144:147", "80:87") SMF("124:127", "144:147", "88:95") SMF("128:131", "144:147", "96:103") "s_nop 7\n\t" \
        FOLD(124, 125, 126, 127, 128, 129, 130, 131) PACK(124, 125, 126, 127) "buffer_store_dwordx4 v[124:127], %[c3], %[ws], %[cs] offen sc1\n\t"
#define RD2(d, a, b) "ds_read2st64_b64 v[" d "], %[a0] offset0:" #a " offset1:" #b "\n\t"
#define MA(o0, o1, om, x0, x1, xm, i, n) "v_mov_b32_dpp v" #o0 ", v164" QP(i) om "\n\tv_mov_b32_dpp v" #o1 ", v165" QP(i) om "\n\tv_mov_b32_dpp v" #x0 ", v166" QP(n) xm "\n\tv_mov_b32_dpp v" #x1 ", v167" QP(n) xm "\n\t"
#define MLO(d0, d1, d2, d3, i, n) MA(d0, d2, "0x5", d1, d3, "0xa", i, n)
#define MHI(d0, d1, d2, d3, i, n) MA(d1, d3, "0xa", d0, d2, "0x5", i, n)
      asm volatile(ZERO4("120:121", "122:123") ZERO4("124:125", "126:127") ZERO4("128:129", "130:131") ZERO4("132:133", "134:135") ZERO4("136:137", "138:139") ZERO4("140:141", "142:143")
        ZERO4("152:153", "154:155") "v_mov_b32 v144, 0x3f80\n\tv_mov_b32 v145, v144\n\tv_mov_b32 v146, v144\n\tv_mov_b32 v147, v144\n\t"
        "v_and_b32 v148, 32, %[ac]\n\tv_cmp_eq_u32 vcc, 0, v148\n\ts_nop 1\n\tv_cndmask_b32 v148, 0, v144, vcc\n\tv_xor_b32 v149, v144, v148\n\tv_mov_b32 v150, v148\n\tv_mov_b32 v151, v149\n\tv_lshlrev_b32 v104, 16, %[xv]\n\tv_and_b32 v105, 0xffff0000, %[xv]\n\t"
        "s_lshr_b32 %[st], %[fl], 8\n\tv_mov_b32 v156, %[st]\n\ts_mov_b32 %[n], %[sp]\n.Lga%=:\n\tds_read_b32 v157, v156\n\ts_sub_u32 %[n], %[n], 1\n\t" LGKM(0)
        "s_cbranch_scc1 .Lgo%=\n\tv_cmp_gt_u32 vcc, %[gn], v157\n\ts_cbranch_vccz .Lgo%=\n\ts_sleep 2\n\ts_branch .Lga%=\n.Lgo%=:\n\ts_memrealtime vcc\n\t" LGKM(0) "s_sub_u32 vcc_lo, vcc_lo, %[tz]\n\ts_cmp_lt_u32 vcc_lo, %[ts]\n\ts_cbranch_scc1 .Lgo%=\n\ts_mov_b32 %[n], vcc_lo\n\ts_mov_b32 %[te], -1\n\tv_mov_b32 v156, 16\n\t"
        WLD("8:11", "vw", 0) WLD("12:15", "vw", 32) WLD("72:75", "v3", 0) WLD("80:83", "v3", 2)
        WLD("16:19", "vw", 2) WLD("20:23", "vw", 34) WLD("76:79", "v3", 32) WLD("84:87", "v3", 34)
        WHEN(2) WLD("24:27", "vw", 4) WLD("28:31", "vw", 36) WLD("88:91", "v3", 1) WLD("96:99", "v3", 3)
        WHEN(3) WLD("32:35", "vw", 6) WLD("36:39", "vw", 38) WLD("92:95", "v3", 33) WLD("100:103", "v3", 35)
        WHEN(4) WLD("40:43", "vw", 8) WLD("44:47", "vw", 40)
        WHEN(5) WLD("48:51", "vw", 10) WLD("52:55", "vw", 42)
        WHEN(6) WLD("56:59", "vw", 12) WLD("60:63", "vw", 44)
        WHEN(7) WLD("64:67", "vw", 14) WLD("68:71", "vw", 46)
        ISSUED CSUM(20, "8:15") CSUM(16, "16:23") CSUM(12, "24:31") CSUM(8, "32:39") LAST64
        CSUM(7, "40:47") CSUM(5, "48:55") CSUM(3, "56:63") CSUM(1, "64:71")
        ZERO4("124:125", "126:127") ZERO4("128:129", "130:131") "s_bitcmp1_b32 %[fl], 0\n\ts_cbranch_scc1 .Lg%=\n\t"
        "v_mov_b32 v156, 24\n\ts_mov_b32 %[n], %[sp]\n.Lcs%=:\n\tds_read_b32 v157, v156\n\tds_read2_b64 v[104:107], %[ac] offset1:32\n\ts_sub_u32 %[n], %[n], 1\n\t" LGKM(0)
        "s_cbranch_scc1 .Ld%=\n\tv_cmp_eq_u32 vcc, 0, v157\n\ts_cbranch_vccz .Ld%=\n\ts_sleep 2\n\ts_branch .Lcs%=\n.Ld%=:\n\t"
        "s_barrier\n\t" "ds_read_b64 v[164:165], %[a1]\n\t" RD2("144:147", 0, 1) RD2("148:151", 2, 3) RD2("152:155", 4, 5) RD2("156:159", 6, 7) RD2("160:163", 8, 9)
        LGKM(5) "v_mul_f32 v166, 0xbfb8aa3b, v165\n\tv_mul_f32 v167, v165, v165\n\t"
        LGKM(4) "v_pk_fma_f32 v[144:145], v[104:105], v[164:165], v[144:145] op_sel_hi:[1,0,1] neg_lo:[1,0,0] neg_hi:[1,0,0]\n\t"
        "v_pk_fma_f32 v[146:147], v[106:107], v[164:165], v[146:147] op_sel_hi:[1,0,1] neg_lo:[1,0,0] neg_hi:[1,0,0]\n\t"
        LGKM(3) PKA("144:145", "144:145", "148:149") PKA("146:147", "146:147", "150:151") LGKM(2) PKA("144:145", "144:145", "152:153") PKA("146:147", "146:147", "154:155")
        LGKM(1) PKA("144:145", "144:145", "156:157") PKA("146:147", "146:147", "158:159") LGKM(0) PKA("144:145", "144:145", "160:161") PKA("146:147", "146:147", "162:163")
        "v_mul_f32 v148, v144, v166\n\tv_mul_f32 v149, v145, v166\n\tv_exp_f32 v148, v148\n\tv_exp_f32 v149, v149\n\tv_pk_mul_f32 v[146:147], v[144:145], v[146:147]\n\t"
        "v_add_f32 v148, 1.0, v148\n\tv_add_f32 v149, 1.0, v149\n\tv_rcp_f32 v148, v148\n\tv_pk_mul_f32 v[146:147], v[146:147], v[166:167] op_sel:[0,1]\n\tv_rcp_f32 v149, v149\n\t"
        "v_mul_f32 v148, v148, v146\n\tv_mul_f32 v149, v149, v147\n\tv_and_b32 v150, 0xffff0000, v148\n\tv_and_b32 v151, 0xffff0000, v149\n\t"
        "v_sub_f32 v148, v148, v150\n\tv_sub_f32 v149, v149, v151\n\tv_cvt_pk_bf16_f32 v164, v150, v148\n\tv_cvt_pk_bf16_f32 v165, v151, v149\n\ts_branch .Lc%=\n"
        ".Lg%=:\n\t" "s_nop 1\n\t" PACK(152, 153, 154, 155) "buffer_store_dwordx4 v[152:155], %[c2], %[ws], %[cs] offen sc1\n\t" "s_barrier\n\t" RD2("144:147", 0, 1) RD2("148:151", 2, 3) RD2("152:155", 4, 5) RD2("156:159", 6, 7)
        LGKM(3) PKA("144:145", "144:145", "146:147") LGKM(2) PKA("148:149", "148:149", "150:151") PKA("144:145", "144:145", "104:105")
        LGKM(1) PKA("152:153", "152:153", "154:155") PKA("144:145", "144:145", "148:149") LGKM(0) PKA("156:157", "156:157", "158:159") PKA("144:145", "144:145", "152:153")
        "s_nop 0\n\tv_add_f32 v144, v144, v156\n\tv_add_f32 v145, v145, v157\n\tv_cvt_pk_bf16_f32 v164, 0, v144\n\tv_cvt_pk_bf16_f32 v165, 0, v145\n\ts_nop 0\n"
        ".Lc%=:\n\tv_mov_b32_dpp v166, v164 row_half_mirror" DPPM "0xf\n\tv_mov_b32_dpp v167, v165 row_half_mirror" DPPM "0xf\n\ts_bitcmp1_b32 %[fl], 1\n\ts_cbranch_scc0 .Lw%=\n\t"
        QUAD(144, 145, 146, 147, 164) QUAD(148, 149, 150, 151, 165) SMF("124:127", "144:147", "72:79") SMF("128:131", "144:147", "80:87") SMF("124:127", "148:151", "88:95") SMF("128:131", "148:151", "96:103")
        ".Lw%=:\n\t" MLO(132, 133, 134, 135, 0, 3) MLO(136, 137, 138, 139, 1, 2) SMF("120:123", "132:135", "8:15") MLO(140, 141, 142, 143, 2, 1) SMF("120:123", "136:139", "16:23")
        MLO(132, 133, 134, 135, 3, 0) SMF("120:123", "140:143", "24:31") MHI(136, 137, 138, 139, 0, 3) SMF("120:123", "132:135", "32:39") MHI(140, 141, 142, 143, 1, 2) SMF("120:123", "136:139", "40:47")
        MHI(132, 133, 134, 135, 2, 1) SMF("120:123", "140:143", "48:55") MHI(136, 137, 138, 139, 3, 0) SMF("120:123", "132:135", "56:63") "s_nop 0\n\t" SMF("120:123", "136:139", "64:71")
        FOLD(124, 125, 126, 127, 128, 129, 130, 131) PACK(120, 121, 122, 123) "buffer_store_dwordx4 v[120:123], %[v2], %[ws], %[po] offen sc1\n\t"
        PACK(124, 125, 126, 127) "buffer_store_dwordx4 v[124:127], %[vo], %[ws], %[po] offen sc1\n\ts_nop 1\n\t"
        : [st] "=&s"(st), [n] "=&s"(n), [te] "=&s"(te)
        : [vw] "v"(vw), [v3] "v"(v3), [rw] "s"(rw), [rs] "s"(rs), [idx] "v"(idx), [xv] "v"(xv),
          [c2] "v"(lo32(lane, v2 + (lane & 8) * (4608 / 8))), [c3] "v"(lo32(lane, vo + 2 * 4608) | sel(w == 3 && isG, 0, 0x80000000u)),
          [cs] "s"(WS_CS + ((xg * 9 + 2 * w) * 9 + rg) * 512 + mat * 256), [v2] "v"(v2 + (lane & 8) * (ps / 8)), [vo] "v"((vo + 2 * ps) | sel(w == 3, 0, 0x80000000u)),
          [a0] "v"(sel(isG, L_A + lane * 8, L_A + ab * 256 + ((ar * 8 + ab * 128) & 255))), [a1] "v"(L_ST + ab * 8), [ac] "v"(L_CS + ar * 8), [ws] "s"(ws),
          [po] "s"(__builtin_amdgcn_readfirstlane(po)), [fl] "s"(__builtin_amdgcn_readfirstlane((isG ? 1 + (GATE << 8) : GATED << 8) + (w == 3 ? 2 : 0))), [sp] "n"(SPIN_L),
          [gn] "s"(isG ? GATEN : GATEDN),
          [tz] "s"((unsigned)tz), [tb] "s"(isG ? TW : TD), [dt] "s"(isG ? DW : DD), [ts] "s"(isG ? TSG : TSD), TAGS
        : "memory", "scc", "vcc", V8(8, 9, 10, 11, 12, 13, 14, 15), V8(16, 17, 18, 19, 20, 21, 22, 23), V8(24, 25, 26, 27, 28, 29, 30, 31), V8(32, 33, 34, 35, 36, 37, 38, 39),
          V8(40, 41, 42, 43, 44, 45, 46, 47), V8(48, 49, 50, 51, 52, 53, 54, 55), V8(56, 57, 58, 59, 60, 61, 62, 63), V8(64, 65, 66, 67, 68, 69, 70, 71), V8(72, 73, 74, 75, 76, 77, 78, 79),
          V8(80, 81, 82, 83, 84, 85, 86, 87), V8(88, 89, 90, 91, 92, 93, 94, 95), V8(96, 97, 98, 99, 100, 101, 102, 103), "v104", "v105", "v106", "v107", "v120", "v121", "v122", "v123",
          V124, V8(148, 149, 150, 151, 152, 153, 154, 155), V8(156, 157, 158, 159, 160, 161, 162, 163), "v164", "v165", "v166", "v167");
      if (isR && w == 0) { int l2 = lane; asm volatile("" : "+v"(l2)); final_sum(ws, out, ri, 8, l2, g); }
    }
  }
  FIN();
}

extern "C" void kernel_launch(void* const* d_in, const int* in_sizes, int n_in, void* d_out, int out_size, void* d_ws, size_t ws_size, hipStream_t stream) {
  fused_decode_kernel<<<256, 768, 0, stream>>>((const u16*)d_in[0], (const u16*)d_in[1], (const u16*)d_in[2], (const u16*)d_in[3], (const u16*)d_in[4],
                                               (const u16*)d_in[6], (const u16*)d_in[7], (const u16*)d_in[8], (u16*)d_out, (unsigned*)d_ws);
}
```
